```python
import torch

def _softmax(x, axis=-1):
    return torch.softmax(x, dim=axis)
def _gelu(x):
    return torch.nn.functional.gelu(x, approximate="tanh")
def _silu(x):
    return torch.nn.functional.silu(x)
def _sigmoid(x):
    return torch.sigmoid(x)
def _layernorm(x, axis=-1, eps=1e-5):
    m = x.mean(dim=axis, keepdim=True)
    v = x.var(dim=axis, keepdim=True, unbiased=False)
    return (x - m) / torch.sqrt(v + eps)
def _scatter_add(tgt, idx, src, axis=0):
    return tgt.clone().index_add_(axis, idx, src)
def _segment_sum(src, seg, n):
    out = torch.zeros((n,) + src.shape[1:], dtype=src.dtype)
    return out.index_add_(0, seg, src)
def _pad(x, pw):
    flat = tuple(v for pair in reversed(pw) for v in pair)
    return torch.nn.functional.pad(x, flat)

kernel_name = 'parallel_sum__sliding_block_cached__swiglu_ffn__decode'

def setup_inputs():

    _w = (lambda a: (lambda t: t.to(torch.bfloat16) if t.is_floating_point() else t)(torch.as_tensor(a)))
    def _A_setup():
        B, Sq, D = 1, 1, 1024
        Hq, Hkv, Sk, Dh = 32, 4, 32768, 128
        assert Hq % Hkv == 0, (Hq, Hkv)
        return {
            "x": _w((lambda *s: torch.randn(*s, dtype=torch.bfloat16) * 0.05)(B, Sq, D)),
            "k_cache": _w((lambda *s: torch.randn(*s, dtype=torch.bfloat16) * 3.2)(B, Hkv, Sk, Dh)),
            "v_cache": _w((lambda *s: torch.randn(*s, dtype=torch.bfloat16) * 1.6)(B, Hkv, Sk, Dh)),
            "wq": _w((lambda *s: torch.randn(*s, dtype=torch.bfloat16) * 3.2)(D, Hq * Dh) * (D ** -0.5)),
            "wo": _w((lambda *s: torch.randn(*s, dtype=torch.bfloat16) * 1.6)(Hq * Dh, D) * ((Hq * Dh) ** -0.5)),
        }
    _A_in = _A_setup()

    def _B_setup():
        B, S, D, F = 1, 1, 1024, 3584
        return {
            "x": _w((lambda *s: torch.randn(*s, dtype=torch.bfloat16) * 0.05)(B, S, D)),
            "w_gate": _w((lambda *s: torch.randn(*s, dtype=torch.bfloat16) * 0.05)(D, F)),
            "w_up": _w((lambda *s: torch.randn(*s, dtype=torch.bfloat16) * 0.05)(D, F)),
            "w_down": _w((lambda *s: torch.randn(*s, dtype=torch.bfloat16) * 0.05)(F, D)),
        }
    _B_in = _B_setup()

    return {
        "A_x": _A_in["x"],
        "A_k_cache": _A_in["k_cache"],
        "A_v_cache": _A_in["v_cache"],
        "A_wq": _A_in["wq"],
        "A_wo": _A_in["wo"],
        "B_x": _B_in["x"],
        "B_w_gate": _B_in["w_gate"],
        "B_w_up": _B_in["w_up"],
        "B_w_down": _B_in["w_down"],
    }

def reference(A_x, A_k_cache, A_v_cache, A_wq, A_wo, B_x, B_w_gate, B_w_up, B_w_down):
    def _A_ref(x, k_cache, v_cache, wq, wo):
        B, Sq, D = x.shape
        Hq, Dh = 32, 128
        Hkv, Sk = k_cache.shape[1], k_cache.shape[2]
        W = min(4096, max(256, Sk // 4))
        rep = Hq // Hkv
        q = (lambda x, *d: x.permute(*d))((x @ wq).reshape(B, Sq, Hq, Dh), 0, 2, 1, 3)
        k = (lambda x, n, axis: torch.repeat_interleave(x, n, dim=axis))(k_cache[:, :, Sk - W:, :], rep, axis=1)
        v = (lambda x, n, axis: torch.repeat_interleave(x, n, dim=axis))(v_cache[:, :, Sk - W:, :], rep, axis=1)
        scores = torch.einsum("bhqd,bhkd->bhqk", q, k) * (Dh ** -0.5)
        p = _softmax(scores, axis=-1)
        o = torch.einsum("bhqk,bhkd->bhqd", p, v)
        return (lambda x, *d: x.permute(*d))(o, 0, 2, 1, 3).reshape(B, Sq, Hq * Dh) @ wo

    def _B_ref(x, w_gate, w_up, w_down):
        g = _silu(x @ w_gate)
        u = x @ w_up
        return (g * u) @ w_down

    x = A_x
    xn = _layernorm(x, axis=-1)
    return x + _A_ref(xn, A_k_cache, A_v_cache, A_wq, A_wo) + _B_ref(xn, B_w_gate, B_w_up, B_w_down)
```

```cpp
#include <hip/hip_runtime.h>
#include <hip/hip_bf16.h>
#include <stdint.h>

#define MIMIC 1
typedef unsigned short u16;
typedef unsigned v4u __attribute__((ext_vector_type(4)));
static __device__ __forceinline__ float bf2f(u16 b){ return __uint_as_float(((unsigned)b)<<16); }
static __device__ __forceinline__ float rbf(float f){
  unsigned u=__float_as_uint(f); u += 0x7fffu + ((u>>16)&1u); return __uint_as_float(u & 0xffff0000u); }
static __device__ __forceinline__ float mrb(float f){ return MIMIC ? rbf(f) : f; }
static __device__ __forceinline__ u16 f2bf(float f){ unsigned u=__float_as_uint(f); u += 0x7fffu + ((u>>16)&1u); return (u16)(u>>16); }

constexpr int D=1024, HQ=32, HKV=4, SK=32768, DH=128, W=4096, F=3584;

__device__ void layernorm_block(const u16* x, float* xn  , float* red){
  int t=threadIdx.x;
  float v[4]; float s=0;
  for(int i=0;i<4;i++){ v[i]=bf2f(x[t*4+i]); s+=v[i]; }
  for(int o=32;o>0;o>>=1) s+=__shfl_xor(s,o);
  if((t&63)==0) red[t>>6]=s;
  __syncthreads();
  float mean=(red[0]+red[1]+red[2]+red[3])/D;
  __syncthreads();
  float q=0; for(int i=0;i<4;i++){ float d=v[i]-mean; q+=d*d; }
  for(int o=32;o>0;o>>=1) q+=__shfl_xor(q,o);
  if((t&63)==0) red[t>>6]=q;
  __syncthreads();
  float var=(red[0]+red[1]+red[2]+red[3])/D;
  float m=mrb(mean); float vv=mrb(var);
  float den=mrb(sqrtf(mrb(vv+1e-5f)));
  for(int i=0;i<4;i++){ xn[t*4+i]=mrb(mrb(v[i]-m)/den); }
  __syncthreads();
}

__global__ void __launch_bounds__(256) k1(const u16* x, const u16* wq, const u16* wg, const u16* wu, float* q, float* g, float* u){
  __shared__ float xn[1024]; __shared__ float red[4]; __shared__ float part[32][65];
  layernorm_block(x,xn,red);
  int b=blockIdx.x; const u16* w; int ld; float* out; int c0;
  if(b<64){ w=wq; ld=4096; out=q; c0=b*64; }
  else if(b<64+56){ w=wg; ld=F; out=g; c0=(b-64)*64; }
  else { w=wu; ld=F; out=u; c0=(b-120)*64; }
  int t=threadIdx.x; int cl=t&7, rg=t>>3;
  float acc[8]={0,0,0,0,0,0,0,0};
  for(int r=rg;r<1024;r+=32){
    v4u v=*(const v4u*)(w+(size_t)r*ld+c0+cl*8);
    float xv=xn[r];
    for(int k=0;k<4;k++){ acc[2*k]+=xv*__uint_as_float(v[k]<<16); acc[2*k+1]+=xv*__uint_as_float(v[k]&0xffff0000u);}
  }
  for(int k=0;k<8;k++) part[rg][cl*8+k]=acc[k];
  __syncthreads();
  if(t<64){ float s=0; for(int r=0;r<32;r++) s+=part[r][t]; out[c0+t]=s; }
}
__global__ void __launch_bounds__(256) k2(const float* q, const u16* kc, const u16* vc, float* o){
  __shared__ float qs[128]; __shared__ float sc[4096]; __shared__ float red[4]; __shared__ float op[2][128];
  int h=blockIdx.x, g=h/8, t=threadIdx.x;
  if(t<128) qs[t]=mrb(q[h*128+t]);
  __syncthreads();
  const u16* kb=kc+((size_t)g*SK+(SK-W))*DH; const u16* vb=vc+((size_t)g*SK+(SK-W))*DH;
  float mx=-INFINITY;
  for(int k=t;k<W;k+=256){ float s=0; for(int d=0;d<128;d+=8){ v4u v=*(const v4u*)(kb+(size_t)k*DH+d); for(int j=0;j<4;j++){ s+=qs[d+2*j]*__uint_as_float(v[j]<<16); s+=qs[d+2*j+1]*__uint_as_float(v[j]&0xffff0000u);} }
    s=mrb(mrb(s)*(MIMIC? 0.08837890625f : 0.08838834764831845f)); sc[k]=s; mx=fmaxf(mx,s);}
  for(int o2=32;o2>0;o2>>=1) mx=fmaxf(mx,__shfl_xor(mx,o2));
  if((t&63)==0) red[t>>6]=mx; __syncthreads();
  mx=fmaxf(fmaxf(red[0],red[1]),fmaxf(red[2],red[3])); __syncthreads();
  float sum=0; for(int k=t;k<W;k+=256){ float e=__expf(sc[k]-mx); sc[k]=e; sum+=e; }
  for(int o2=32;o2>0;o2>>=1) sum+=__shfl_xor(sum,o2);
  if((t&63)==0) red[t>>6]=sum; __syncthreads();
  sum=red[0]+red[1]+red[2]+red[3];
  int d=t&127, half=t>>7; float acc=0;
  for(int k=half;k<W;k+=2){ acc+=mrb(sc[k]/sum)*bf2f(vb[(size_t)k*DH+d]); }
  op[half][d]=acc; __syncthreads();
  if(t<128) o[h*128+t]=mrb(op[0][t]+op[1][t]);
}
__global__ void __launch_bounds__(256) k3(const float* o, const float* g, const float* u, const u16* wo, const u16* wd, float* part){
  __shared__ float in[256]; __shared__ float pr[32][65];
  int cb=blockIdx.x&15, rb=blockIdx.x>>4; int t=threadIdx.x; const u16* w;
  if(rb<16){ in[t]=o[rb*256+t]; w=wo+(size_t)rb*256*1024; }
  else { int r=(rb-16)*256+t; float gv=mrb(g[r]); float s=mrb(gv/(1.f+__expf(-gv))); in[t]=mrb(s*mrb(u[r])); w=wd+(size_t)(rb-16)*256*1024; }
  __syncthreads();
  int cl=t&7, rg=t>>3; float acc[8]={0,0,0,0,0,0,0,0};
  for(int r=rg;r<256;r+=32){ v4u v=*(const v4u*)(w+(size_t)r*1024+cb*64+cl*8); float xv=in[r];
    for(int k=0;k<4;k++){ acc[2*k]+=xv*__uint_as_float(v[k]<<16); acc[2*k+1]+=xv*__uint_as_float(v[k]&0xffff0000u);} }
  for(int k=0;k<8;k++) pr[rg][cl*8+k]=acc[k];
  __syncthreads();
  if(t<64){ float s=0; for(int r=0;r<32;r++) s+=pr[r][t]; part[rb*1024+cb*64+t]=s; }
}
__global__ void k4(const u16* x, const float* part, u16* out){
  int c=blockIdx.x*256+threadIdx.x; float a=0,f=0;
  for(int r=0;r<16;r++) a+=part[r*1024+c];
  for(int r=16;r<30;r++) f+=part[r*1024+c];
  float y=mrb(bf2f(x[c])+mrb(a)); y=y+mrb(f);
  out[c]=f2bf(y);
}
extern "C" void kernel_launch(void* const* d_in, const int* in_sizes, int n_in, void* d_out, int out_size, void* d_ws, size_t ws_size, hipStream_t stream){
  const u16* x=(const u16*)d_in[0]; const u16* kc=(const u16*)d_in[1]; const u16* vc=(const u16*)d_in[2];
  const u16* wq=(const u16*)d_in[3]; const u16* wo=(const u16*)d_in[4];
  const u16* wg=(const u16*)d_in[6]; const u16* wu=(const u16*)d_in[7]; const u16* wd=(const u16*)d_in[8];
  float* ws=(float*)d_ws; float* q=ws; float* g=ws+4096; float* u=ws+8192; float* o=ws+12288; float* part=ws+16384;
  k1<<<64+56+56,256,0,stream>>>(x,wq,wg,wu,q,g,u);
  k2<<<32,256,0,stream>>>(q,kc,vc,o);
  k3<<<16*30,256,0,stream>>>(o,g,u,wo,wd,part);
  k4<<<4,256,0,stream>>>(x,part,(u16*)d_out);
}
```

```cpp
#include <hip/hip_runtime.h>
#include <stdint.h>

#define STAMPS 0
#define WIN 12
#define SLP 1
#define GSLP 4
#define CPRIO 1
#define NT_AUX " nt"

typedef unsigned short u16;
typedef unsigned long long u64;
typedef unsigned v4u __attribute__((ext_vector_type(4)));
typedef unsigned v2u __attribute__((ext_vector_type(2)));
typedef float f4 __attribute__((ext_vector_type(4)));
typedef short s4 __attribute__((ext_vector_type(4)));
typedef __bf16 b8 __attribute__((ext_vector_type(8)));
typedef __bf16 b2 __attribute__((ext_vector_type(2)));
#define GAS __attribute__((address_space(1)))
#define LAS __attribute__((address_space(3)))
#define RLX_AGENT __ATOMIC_RELAXED, __HIP_MEMORY_SCOPE_AGENT
#define DEV __device__ __forceinline__

constexpr int SK = 32768, WKEYS = 4096, FF = 3584;
constexpr int WS_EPOCH = 0, WS_READY = 4096;
constexpr int WS_QP = WS_READY + 256 * 128;
constexpr int OPS = 384;
constexpr int WS_OP = WS_QP + 32 * 8 * 512;
constexpr int WS_GU = WS_OP + 32 * 64 * OPS;
constexpr int WS_FP = WS_GU + 28 * 8 * 512;
constexpr int WS_END = WS_FP + 32 * 32 * 128;
constexpr int WS_STAMP = 2 << 20;
constexpr unsigned SENT = 0xFFFFFFFFu;
constexpr int R0 = 0, R1 = 32768, R2 = 65536, R3 = 98304;
constexpr int L_XN = 131072;
constexpr int L_XR = L_XN + 2048;
constexpr int L_Q = L_XR + 2048;
constexpr int L_OW = L_Q + 8 * 288;
constexpr int L_ML = L_OW + 32 * 528;
constexpr int L_WT = L_ML + 256;
constexpr int L_OV = L_WT + 512;
constexpr int L_HV = L_OV + 256;
constexpr int L_FP = L_HV + 256;
constexpr int L_FLG = L_FP + 512;
constexpr int LDS_BYTES = L_FLG + 128;
static_assert(LDS_BYTES <= 160 * 1024, "lds");
enum { F_LAND0 = 0, F_LAND1 = 1, F_XN = 2, F_CLR = 3, F_GOK = 4, F_QC = 5, F_OC = 6, F_OV = 7, F_WQD = 8, F_GU0 = 9, F_HV = 10, F_FFN = 11, F_TAGLO = 12, F_TAGHI = 13, F_XR0 = 14, F_XR1 = 15 };

constexpr unsigned CODE_BYTES = 18432; constexpr int CODE_CHUNKS = (CODE_BYTES + 8191) / 8192;
constexpr unsigned SPIN_LDS = 1u << 22, SPIN_GLB = 1u << 18;

DEV float bf_lo(unsigned v) { return __uint_as_float(v << 16); }
DEV float bf_hi(unsigned v) { return __uint_as_float(v & 0xffff0000u); }
DEV unsigned pk_bf16(float lo, float hi) {
  b2 r; r[0] = (__bf16)lo; r[1] = (__bf16)hi; return __builtin_bit_cast(unsigned, r);
}
DEV float rbf(float x) { return __uint_as_float(pk_bf16(x, x) << 16); }
DEV unsigned nosent(unsigned v) { return v < 0xFFFFFFFEu ? v : 0xFFFFFFFEu; }
template <int CTRL> DEV float dpp(float x) {
  return __builtin_bit_cast(float, __builtin_amdgcn_mov_dpp(__builtin_bit_cast(int, x), CTRL, 0xf, 0xf, true));
}
constexpr int XOR1 = 0xB1, XOR2 = 0x4E, HMIR = 0x141, ROR8 = 0x128, ROR4 = 0x124;
DEV float x16_sum(float x) {
  auto s = __builtin_amdgcn_permlane16_swap(__float_as_uint(x), __float_as_uint(x), false, false);
  return __uint_as_float(s[0]) + __uint_as_float(s[1]);
}
DEV float x32_sum(float x) {
  auto s = __builtin_amdgcn_permlane32_swap(__float_as_uint(x), __float_as_uint(x), false, false);
  return __uint_as_float(s[0]) + __uint_as_float(s[1]);
}
DEV float x16_max(float x) {
  auto s = __builtin_amdgcn_permlane16_swap(__float_as_uint(x), __float_as_uint(x), false, false);
  return fmaxf(__uint_as_float(s[0]), __uint_as_float(s[1]));
}
DEV float x32_max(float x) {
  auto s = __builtin_amdgcn_permlane32_swap(__float_as_uint(x), __float_as_uint(x), false, false);
  return fmaxf(__uint_as_float(s[0]), __uint_as_float(s[1]));
}
DEV float wave_sum(float x) {
  x += dpp<XOR1>(x); x += dpp<XOR2>(x); x += dpp<HMIR>(x); x += dpp<ROR8>(x);
  return x32_sum(x16_sum(x));
}
DEV float wave_max(float x) {
  x = fmaxf(x, dpp<XOR1>(x)); x = fmaxf(x, dpp<XOR2>(x)); x = fmaxf(x, dpp<HMIR>(x)); x = fmaxf(x, dpp<ROR8>(x));
  return x32_max(x16_max(x));
}
#define CFENCE() asm volatile("" ::: "memory")
#define BARRIER() asm volatile("s_barrier" ::: "memory")
#define LGKM0() asm volatile("s_waitcnt lgkmcnt(0)" ::: "memory")
template <int N> DEV void waitvm() { asm volatile("s_waitcnt vmcnt(%0)" ::"n"(N) : "memory"); }

typedef LAS unsigned char* lptr;
DEV volatile LAS unsigned* flg(lptr L, int i) { return (volatile LAS unsigned*)(L + L_FLG) + i; }
DEV void wait_flag(lptr L, int i, unsigned v) {
  for (unsigned n = 0; n < SPIN_LDS; ++n) {
    if ((unsigned)__builtin_amdgcn_readfirstlane(*flg(L, i)) >= v) break;
    __builtin_amdgcn_s_sleep(SLP);
  }
  CFENCE();
}
DEV void wait_land(lptr L, unsigned v, int pair = 0) {
  for (unsigned n = 0; n < SPIN_LDS; ++n) {
    const v2u f = *(volatile LAS v2u*)(L + L_FLG + pair * 4);
    if ((unsigned)__builtin_amdgcn_readfirstlane(f.x < f.y ? f.x : f.y) >= v) break;
    __builtin_amdgcn_s_sleep(SLP);
  }
  CFENCE();
}
DEV void set_flag(lptr L, int i, unsigned v) { LGKM0(); *flg(L, i) = v; }
DEV void inc_flag(lptr L, int i, int lane) {
  LGKM0();
  if (lane == 0) __hip_atomic_fetch_add((LAS unsigned*)(L + L_FLG) + i, 1u, __ATOMIC_RELAXED, __HIP_MEMORY_SCOPE_WORKGROUP);
}
typedef __amdgpu_buffer_rsrc_t rsrc_t;
DEV v4u ld16(rsrc_t rs, int off) { return __builtin_amdgcn_raw_buffer_load_b128(rs, off, 0, 16); }
DEV v2u ld8(rsrc_t rs, int off) { return __builtin_amdgcn_raw_buffer_load_b64(rs, off, 0, 16); }
DEV unsigned ld4(rsrc_t rs, int off) { return __builtin_amdgcn_raw_buffer_load_b32(rs, off, 0, 16); }
DEV void st16(rsrc_t rs, int off, v4u v) { __builtin_amdgcn_raw_buffer_store_b128(v, rs, off, 0, 16); }
DEV void st8(rsrc_t rs, int off, v2u v) { __builtin_amdgcn_raw_buffer_store_b64(v, rs, off, 0, 16); }
DEV void st4(rsrc_t rs, int off, unsigned v) { __builtin_amdgcn_raw_buffer_store_b32(v, rs, off, 0, 16); }
DEV v4u f4_pub(f4 a) {
  return v4u{nosent(__float_as_uint(a[0])), nosent(__float_as_uint(a[1])), nosent(__float_as_uint(a[2])), nosent(__float_as_uint(a[3]))};
}
DEV f4 as_f4(v4u v) { return __builtin_bit_cast(f4, v); }

DEV unsigned max4(v4u v) { const unsigned a = v.x > v.y ? v.x : v.y, b = v.z > v.w ? v.z : v.w; return a > b ? a : b; }
DEV unsigned umax(unsigned a, unsigned b) { return a > b ? a : b; }
template <int S> DEV void ldx4(const unsigned char* ws, unsigned off, v4u& a, v4u& b, v4u& c, v4u& d) {
  constexpr int B = S > 1024 ? -S / 2 : 0;
  asm volatile("global_load_dwordx4 %0, %4, %5 offset:%9 sc1\n\tglobal_load_dwordx4 %1, %4, %5 offset:%6 sc1\n\t"
               "global_load_dwordx4 %2, %4, %5 offset:%7 sc1\n\tglobal_load_dwordx4 %3, %4, %5 offset:%8 sc1"
               : "+v"(a), "+v"(b), "+v"(c), "+v"(d) : "v"(off), "s"(ws), "n"(B + S), "n"(B + 2 * S), "n"(B + 3 * S), "n"(B) : "memory");
}
#define VM0_4(v, o) asm volatile("s_waitcnt vmcnt(0)" : "+v"(v[o]), "+v"(v[o + 1]), "+v"(v[o + 2]), "+v"(v[o + 3]) :: "memory")
#define PIN_4(v, o) asm volatile("" : "+v"(v[o]), "+v"(v[o + 1]), "+v"(v[o + 2]), "+v"(v[o + 3]) :: "memory")
template <int N, int S, bool X> DEV void gather(const unsigned char* ws, unsigned off, v4u (&v)[N], unsigned xoff, unsigned& xv) {
#pragma unroll
  for (int i = 0; i < N; ++i) v[i] = v4u{SENT, SENT, SENT, SENT};
  bool bad = true;
  for (unsigned n = 0; n < SPIN_GLB; ++n) {
    if (bad) {
      if (X) asm volatile("global_load_dword %0, %1, %2 sc1" : "+v"(xv) : "v"(xoff), "s"(ws) : "memory");
#pragma unroll
      for (int i = 0; i < N; i += 4) ldx4<S>(ws, off + i * S, v[i], v[i + 1], v[i + 2], v[i + 3]);
      VM0_4(v, 0);
#pragma unroll
      for (int i = 4; i < N; i += 4) PIN_4(v, i);
      if (X) asm volatile("" : "+v"(xv));
      unsigned mx = X ? xv : 0u;
#pragma unroll
      for (int i = 0; i < N; ++i) mx = umax(mx, max4(v[i]));
      bad = mx == SENT;
    }
    if (!__any(bad)) break;
    __builtin_amdgcn_s_sleep(GSLP);
  }
}

template <int NCB> DEV void gemv_half(lptr L, int tb, int xb, int c0, int lane, f4 (&acc)[NCB]) {
  const int g = lane >> 4, q = (lane >> 2) & 3, p = lane & 3;
  const int rr = 4 * (g & 1) + q;
  const int base = tb + 256 * (4 * g + q) + 8 * p;
#pragma unroll
  for (int ks = 0; ks < 2; ++ks) {
    const v2u x0 = *(LAS v2u*)(L + xb + 64 * ks + 8 * g), x1 = *(LAS v2u*)(L + xb + 64 * ks + 32 + 8 * g);
    const v4u xf = {x0.x, x0.y, x1.x, x1.y};
#pragma unroll
    for (int i = 0; i < NCB; ++i) {
      const int a = base + (((c0 + i) ^ rr) << 5) + 8192 * ks;
      const v2u t0 = __builtin_bit_cast(v2u, __builtin_amdgcn_ds_read_tr16_b64_v4i16((LAS s4*)(L + a)));
      const v2u t1 = __builtin_bit_cast(v2u, __builtin_amdgcn_ds_read_tr16_b64_v4i16((LAS s4*)(L + a + 4096)));
      const v4u wf = {t0.x, t0.y, t1.x, t1.y};
      acc[i] = __builtin_amdgcn_mfma_f32_16x16x32_bf16(__builtin_bit_cast(b8, wf), __builtin_bit_cast(b8, xf), acc[i], 0, 0, 0);
    }
  }
}

template <int STRIDE, bool WAIT> DEV void issue_half(lptr L, int j, const void* base, unsigned vbase, unsigned r4, unsigned lds, int pub) {
  unsigned voff = vbase + r4 * STRIDE + j * 4 * STRIDE;
  unsigned m = (unsigned)(size_t)L + lds + j * 1024;
#pragma unroll
  for (int k = 0; k < 8; ++k) {
    asm volatile("s_mov_b32 m0, %0\n\ts_nop 0\n\tglobal_load_lds_dwordx4 %1, %2" NT_AUX ::"s"(m), "v"(voff), "s"(base) : "memory");
    voff += 8 * STRIDE; m += 2048;
    if (WAIT) waitvm<WIN - 1>();
    if (k == (WIN - 2) % 8 && pub > 0) *flg(L, j) = (unsigned)pub;
  }
}

#define STAMP(i)
#define STAMP_OUT()

__global__ void __launch_bounds__(512, 2)
fused_decode(const u16* __restrict__ x, const u16* __restrict__ kc, const u16* __restrict__ vc, const u16* __restrict__ wq,
             const u16* __restrict__ wo, const u16* __restrict__ wg, const u16* __restrict__ wu, const u16* __restrict__ wd,
             u16* __restrict__ out, unsigned char* ws) {
  extern __shared__ __attribute__((aligned(16))) unsigned char smem[];
  lptr L = (lptr)smem;
  const int c = blockIdx.x, tid = threadIdx.x, lane = tid & 63, wave = __builtin_amdgcn_readfirstlane(tid >> 6);
  STAMP(0);
  u64 pc0; asm volatile("s_getpc_b64 %0" : "=s"(pc0));
  const int g = (c & 7) >> 1, m = (c & 1) * 32 + (c >> 3), hh = m >> 3, j8 = m & 7, h = 8 * g + hh;
  const bool has_gu = hh != 0, has_wd = hh != 7;
  const unsigned n_gu = 4, n_wd = has_gu ? 8 : 4, n_wo = n_wd + (has_wd ? 2 : 0), n_all = n_wo + 2;
  const int wo_reg = has_wd ? R2 : R0;
  constexpr int LAG = (WIN - 2) / 8 + 1;

  if (__builtin_expect(wave < 2, 1)) {
    const int j = wave;
    const unsigned r4 = lane >> 4;
    const unsigned sw = ((lane & 15) ^ ((4 * j + r4) << 1)) << 4;
    const char* bq = (const char*)wq + (size_t)j8 * 128 * 8192 + h * 256;
    __builtin_amdgcn_s_setprio(3);
    asm volatile("s_mov_b32 m0, %0\n\ts_nop 0\n\tglobal_load_lds_dwordx4 %1, %2" ::"s"((unsigned)(size_t)L + L_XR + j * 1024), "v"(lane * 16 + j * 1024), "s"(x) : "memory");
    issue_half<8192, false>(L, j, bq, sw, r4, R0, 0);
    issue_half<8192, false>(L, j, bq, sw + 64 * 8192, r4, R0 + 16384, 0);
    BARRIER();
    waitvm<16>(); *flg(L, F_XR0 + j) = 1u;
    const size_t kvo = ((size_t)g * SK + (SK - WKEYS) + m * 64) * 256;
    int i = 2;
    issue_half<256, true>(L, j, (const char*)kc + kvo, sw, r4, R1, i - LAG + 1); ++i;
    STAMP(1);
    issue_half<256, true>(L, j, (const char*)vc + kvo, sw, r4, R1 + 16384, i - LAG + 1); ++i;
    STAMP(2);
    if (has_gu) {
      const int cbg = 7 * g + hh - 1, rbg = j8 & 3;
      const char* bg = (const char*)((j8 & 4) ? wu : wg) + (size_t)rbg * 256 * 7168 + cbg * 256;
      issue_half<7168, true>(L, j, bg, sw, r4, R2, i - LAG + 1); ++i;
      issue_half<7168, true>(L, j, bg, sw + 64 * 7168, r4, R2 + 16384, i - LAG + 1); ++i;
      issue_half<7168, true>(L, j, bg, sw + 128 * 7168, r4, R3, i - LAG + 1); ++i;
      issue_half<7168, true>(L, j, bg, sw + 192 * 7168, r4, R3 + 16384, i - LAG + 1); ++i;
    }
    STAMP(3);
    wait_flag(L, F_WQD, 4);
    if (has_wd) {
      const char* bd = (const char*)wd + (size_t)(7 * g + hh) * 128 * 2048 + j8 * 256;
      issue_half<2048, true>(L, j, bd, sw, r4, R0, i - LAG + 1); ++i;
      issue_half<2048, true>(L, j, bd, sw + 64 * 2048, r4, R0 + 16384, i - LAG + 1); ++i;
      if (has_gu) wait_flag(L, F_GU0, 2);
    }
    STAMP(4);
    const char* bo = (const char*)wo + (size_t)h * 128 * 2048 + j8 * 256;
    issue_half<2048, true>(L, j, bo, sw, r4, wo_reg, i - LAG + 1); ++i;
    issue_half<2048, true>(L, j, bo, sw + 64 * 2048, r4, wo_reg + 16384, i - LAG + 1); ++i;
    STAMP(5);
    if (WIN > 17) { waitvm<16>(); *flg(L, j) = n_all - 2; }
    waitvm<8>(); *flg(L, j) = n_all - 1;
    waitvm<0>(); *flg(L, j) = n_all;
    STAMP(6);
    STAMP_OUT();
    return;
  }

  const rsrc_t rs = __builtin_amdgcn_make_buffer_rsrc(ws, 0, WS_END, 0x00020000);
  if (wave == 7) {
    if (lane < 32) *flg(L, lane) = 0u;
    LGKM0();
  }
  unsigned pf0 = 0;
  if (wave < 2 + CODE_CHUNKS) {
    const unsigned o = (wave - 2) * 8192 + lane * 128;
    pf0 = *(const GAS unsigned*)((pc0 & ~127ull) + (o < CODE_BYTES ? o : 0));
  }

  if (wave >= 6) {
    const int f = wave - 6;
    f4 xres = {0.f, 0.f, 0.f, 0.f};
    if (f == 0) {
      u64 tag;
      asm volatile("s_load_dwordx2 %0, %1, 0x0\n\ts_waitcnt lgkmcnt(0)" : "=s"(tag) : "s"(ws) : "memory");
      tag += 1;
      const v4u sv = {SENT, SENT, SENT, SENT};
      const int l5 = lane & 31;
      st16(rs, lane < 32 ? WS_QP + (h * 8 + j8) * 512 + l5 * 16 : WS_FP + ((j8 * 4 + (l5 >> 3)) * 32 + h) * 128 + (l5 & 7) * 16, sv);
      st16(rs, WS_OP + ((8 * g + (lane >> 4)) * 64 + m) * OPS + (lane & 15) * 16, sv);
      st16(rs, WS_OP + ((8 * g + 4 + (lane >> 4)) * 64 + m) * OPS + (lane & 15) * 16, sv);
      if (lane < 8) st4(rs, WS_OP + ((8 * g + lane) * 64 + m) * OPS + 256, SENT);
      if (has_gu && lane < 32) st16(rs, WS_GU + ((7 * g + hh - 1) * 8 + j8) * 512 + lane * 16, sv);
      BARRIER();
      waitvm<0>();
      if (lane == 0) __hip_atomic_store((GAS u64*)(ws + WS_READY) + (g * 64 + m) * 16, tag, RLX_AGENT);
      *flg(L, F_TAGLO) = (unsigned)tag; *flg(L, F_TAGHI) = (unsigned)(tag >> 32);
      set_flag(L, F_CLR, 1);
      STAMP(1);
      wait_land(L, 1);
      {
        bool bad = true;
        for (unsigned n = 0; n < SPIN_GLB; ++n) {
          if (bad) bad = __hip_atomic_load((GAS u64*)(ws + WS_READY) + (g * 64 + lane) * 16, RLX_AGENT) != tag;
          if (!__any(bad)) break;
          __builtin_amdgcn_s_sleep(GSLP);
        }
      }
      set_flag(L, F_GOK, 1);
      STAMP(2);
    } else {
      BARRIER();
      wait_land(L, 1, F_XR0);
      STAMP(2);
      const v4u xa = *(LAS v4u*)(L + L_XR + lane * 32), xb = *(LAS v4u*)(L + L_XR + lane * 32 + 16);
      {
        const v2u xr = *(LAS v2u*)(L + L_XR + (j8 * 128 + g * 32 + (lane & 7) * 4) * 2);
        xres = f4{bf_lo(xr.x), bf_hi(xr.x), bf_lo(xr.y), bf_hi(xr.y)};
      }
      float v[16];
#pragma unroll
      for (int k = 0; k < 4; ++k) { v[2 * k] = bf_lo(xa[k]); v[2 * k + 1] = bf_hi(xa[k]); v[8 + 2 * k] = bf_lo(xb[k]); v[9 + 2 * k] = bf_hi(xb[k]); }
      float s = 0.f;
#pragma unroll
      for (int k = 0; k < 16; ++k) s += v[k];
      const float mean = wave_sum(s) * (1.f / 1024.f);
      float q2 = 0.f;
#pragma unroll
      for (int k = 0; k < 16; ++k) { const float d = v[k] - mean; q2 = fmaf(d, d, q2); }
      const float var = wave_sum(q2) * (1.f / 1024.f);
      const float mb = rbf(mean), rden = 1.0f / rbf(__builtin_sqrtf(rbf(rbf(var) + 1e-5f)));
      v4u o0, o1;
#pragma unroll
      for (int k = 0; k < 4; ++k) {
        o0[k] = pk_bf16(rbf(v[2 * k] - mb) * rden, rbf(v[2 * k + 1] - mb) * rden);
        o1[k] = pk_bf16(rbf(v[8 + 2 * k] - mb) * rden, rbf(v[9 + 2 * k] - mb) * rden);
      }
      *(LAS v4u*)(L + L_XN + lane * 32) = o0;
      *(LAS v4u*)(L + L_XN + lane * 32 + 16) = o1;
      set_flag(L, F_XN, 1);
      STAMP(1);
    }
    if (has_gu) {
      wait_flag(L, F_XN, 1);
      f4 acc[4];
#pragma unroll
      for (int i = 0; i < 4; ++i) acc[i] = f4{0.f, 0.f, 0.f, 0.f};
      const int xb = L_XN + (j8 & 3) * 512;
      wait_land(L, n_gu + 1); gemv_half<4>(L, R2, xb, 4 * f, lane, acc);
      wait_land(L, n_gu + 2); gemv_half<4>(L, R2 + 16384, xb + 128, 4 * f, lane, acc);
      asm volatile("" :: "v"(acc[0]), "v"(acc[1]), "v"(acc[2]), "v"(acc[3]));
      inc_flag(L, F_GU0, lane);
      STAMP(3);
      wait_land(L, n_gu + 3); gemv_half<4>(L, R3, xb + 256, 4 * f, lane, acc);
      wait_land(L, n_gu + 4); gemv_half<4>(L, R3 + 16384, xb + 384, 4 * f, lane, acc);
      const int n = lane & 15;
      const f4 r = n == 0 ? acc[0] : n == 1 ? acc[1] : n == 2 ? acc[2] : acc[3];
      wait_flag(L, F_CLR, 1);
      if (n < 4) st16(rs, WS_GU + ((7 * g + hh - 1) * 8 + j8) * 512 + ((4 * f + n) * 16 + (lane >> 4) * 4) * 4, f4_pub(r));
      STAMP(4);
    }
    if (has_wd) {
      if (f == 0) {
        v4u v[4];
        unsigned dummy = 0; gather<4, 512, false>(ws, WS_GU + ((7 * g + hh) * 8 + (lane >> 5) * 4) * 512 + (lane & 31) * 16, v, 0, dummy);
        STAMP(5);
        const f4 s = (as_f4(v[0]) + as_f4(v[1])) + (as_f4(v[2]) + as_f4(v[3]));
        float hv[4];
#pragma unroll
        for (int k = 0; k < 4; ++k) {
          auto sw = __builtin_amdgcn_permlane32_swap(__float_as_uint(s[k]), __float_as_uint(s[k]), false, false);
          const float gt = rbf(__uint_as_float(sw[0])), up = rbf(__uint_as_float(sw[1]));
          const float sg = rbf(gt * __builtin_amdgcn_rcpf(1.f + __expf(-gt)));
          hv[k] = sg * up;
        }
        if (lane < 32) *(LAS v2u*)(L + L_HV + lane * 8) = v2u{pk_bf16(hv[0], hv[1]), pk_bf16(hv[2], hv[3])};
        set_flag(L, F_HV, 1);
      } else {
        wait_flag(L, F_HV, 1);
      }
      f4 acc[4];
#pragma unroll
      for (int i = 0; i < 4; ++i) acc[i] = f4{0.f, 0.f, 0.f, 0.f};
      wait_land(L, n_wd + 1); gemv_half<4>(L, R0, L_HV, 4 * f, lane, acc);
      wait_land(L, n_wd + 2); gemv_half<4>(L, R0 + 16384, L_HV + 128, 4 * f, lane, acc);
      const int n = lane & 15;
      const f4 r = n == 0 ? acc[0] : n == 1 ? acc[1] : n == 2 ? acc[2] : acc[3];
      if (n < 4) *(LAS f4*)(L + L_FP + ((4 * f + n) * 16 + (lane >> 4) * 4) * 4) = r;
      inc_flag(L, F_FFN, lane);
      STAMP(6);
    }
    if (hh == 0 && f == 1) {
      wait_flag(L, F_CLR, 1);
      const u64 tag = (u64)*flg(L, F_TAGLO) | ((u64)*flg(L, F_TAGHI) << 32);
      for (unsigned n = 0; n < SPIN_GLB; ++n) {
        bool ok = true;
#pragma unroll
        for (int i = 0; i < 4; ++i) ok &= __hip_atomic_load((GAS u64*)(ws + WS_READY) + (i * 64 + lane) * 16, RLX_AGENT) == tag;
        if (__all(ok)) break;
        __builtin_amdgcn_s_sleep(GSLP);
      }
      CFENCE();
      if (c == 0 && lane == 0) __hip_atomic_store((GAS u64*)(ws + WS_EPOCH), tag, RLX_AGENT);
      STAMP(7);
      v4u v[4];
      unsigned dummy = 0; gather<4, 1024, false>(ws, WS_FP + (j8 * 4 + g) * 4096 + lane * 16, v, 0, dummy);
      STAMP(8);
      f4 s = (as_f4(v[0]) + as_f4(v[1])) + (as_f4(v[2]) + as_f4(v[3]));
#pragma unroll
      for (int k = 0; k < 4; ++k) { float t = s[k]; t += dpp<ROR8>(t); s[k] = x32_sum(x16_sum(t)) + xres[k]; }
      if (lane < 8) *(GAS v2u*)(out + j8 * 128 + g * 32 + lane * 4) = v2u{pk_bf16(s[0], s[1]), pk_bf16(s[2], s[3])};
      STAMP(9);
    }
    STAMP_OUT();
    return;
  }

  const int w = wave - 2, n16 = lane & 15, g4 = lane >> 4;
  BARRIER();
  asm volatile("" :: "v"(pf0));
  __builtin_amdgcn_s_setprio(CPRIO);
  {
    f4 acc[2] = {f4{0.f, 0.f, 0.f, 0.f}, f4{0.f, 0.f, 0.f, 0.f}};
    wait_flag(L, F_XN, 1);
    wait_land(L, 1); gemv_half<2>(L, R0, L_XN + j8 * 256, 2 * w, lane, acc);
    wait_flag(L, F_CLR, 1);
    wait_land(L, 2);
    STAMP(1);
    gemv_half<2>(L, R0 + 16384, L_XN + j8 * 256 + 128, 2 * w, lane, acc);
    const f4 r = n16 == 0 ? acc[0] : acc[1];
    if (n16 < 2) st16(rs, WS_QP + (h * 8 + j8) * 512 + ((2 * w + n16) * 16 + g4 * 4) * 4, f4_pub(r));
    inc_flag(L, F_WQD, lane);
    STAMP(2);
  }
  {
    wait_flag(L, F_GOK, 1);
    v4u v[8];
    unsigned dummy = 0; gather<8, 1024, false>(ws, WS_QP + (8 * g + 2 * w) * 4096 + lane * 16, v, 0, dummy);
    STAMP(3);
    const f4 a = (as_f4(v[0]) + as_f4(v[1])) + (as_f4(v[2]) + as_f4(v[3]));
    const f4 b = (as_f4(v[4]) + as_f4(v[5])) + (as_f4(v[6]) + as_f4(v[7]));
    float qv[4];
#pragma unroll
    for (int k = 0; k < 4; ++k) {
      auto sw = __builtin_amdgcn_permlane32_swap(__float_as_uint(a[k]), __float_as_uint(b[k]), false, false);
      qv[k] = __uint_as_float(sw[0]) + __uint_as_float(sw[1]);
    }
    *(LAS v2u*)(L + L_Q + (2 * w + (lane >> 5)) * 288 + (lane & 31) * 8) = v2u{pk_bf16(qv[0], qv[1]), pk_bf16(qv[2], qv[3])};
    inc_flag(L, F_QC, lane);
  }
  {
    wait_land(L, 3);
    v4u kf[4];
    const int krow = 16 * w + n16;
#pragma unroll
    for (int ks = 0; ks < 4; ++ks) kf[ks] = *(LAS v4u*)(L + R1 + krow * 256 + (((4 * ks + g4) ^ ((n16 & 7) << 1)) << 4));
    wait_flag(L, F_QC, 4);
    STAMP(4);
    f4 s = {0.f, 0.f, 0.f, 0.f};
#pragma unroll
    for (int ks = 0; ks < 4; ++ks) {
      const v4u qf = *(LAS v4u*)(L + L_Q + (n16 & 7) * 288 + ks * 64 + g4 * 16);
      s = __builtin_amdgcn_mfma_f32_16x16x32_bf16(__builtin_bit_cast(b8, kf[ks]), __builtin_bit_cast(b8, qf), s, 0, 0, 0);
    }
    float sc[4];
#pragma unroll
    for (int r = 0; r < 4; ++r) sc[r] = rbf(rbf(s[r]) * 0.08838834764831845f);
    const float mw = x32_max(x16_max(fmaxf(fmaxf(sc[0], sc[1]), fmaxf(sc[2], sc[3]))));
    float p[4];
#pragma unroll
    for (int r = 0; r < 4; ++r) p[r] = __builtin_amdgcn_exp2f((sc[r] - mw) * 1.4426950408889634f);
    const float lw = x32_sum(x16_sum((p[0] + p[1]) + (p[2] + p[3])));
    const v2u pf = {pk_bf16(p[0], p[1]), pk_bf16(p[2], p[3])};
    wait_land(L, 4);
    const int q = (lane >> 2) & 3, pp = lane & 3, rr = 4 * (g4 & 1) + q;
    const int vb = R1 + 16384 + (16 * w + 4 * g4 + q) * 256 + 8 * pp;
    if (lane < 8) *(LAS v2u*)(L + L_ML + (lane * 4 + w) * 8) = v2u{__float_as_uint(mw), __float_as_uint(lw)};
#pragma unroll
    for (int db = 0; db < 8; ++db) {
      const s4 vf = __builtin_amdgcn_ds_read_tr16_b64_v4i16((LAS s4*)(L + vb + ((db ^ rr) << 5)));
      const f4 o = __builtin_amdgcn_mfma_f32_16x16x16bf16_1k(vf, __builtin_bit_cast(s4, pf), f4{0.f, 0.f, 0.f, 0.f}, 0, 0, 0);
      if (n16 < 8) *(LAS f4*)(L + L_OW + (w * 8 + n16) * 528 + (db * 16 + g4 * 4) * 4) = o;
    }
    inc_flag(L, F_OC, lane);
    wait_flag(L, F_OC, 4);
    STAMP(5);
    const int hd = 2 * w + (lane >> 5);
    const v4u ml0 = *(LAS v4u*)(L + L_ML + hd * 32), ml1 = *(LAS v4u*)(L + L_ML + hd * 32 + 16);
    const float m0 = __uint_as_float(ml0.x), m1 = __uint_as_float(ml0.z), m2 = __uint_as_float(ml1.x), m3 = __uint_as_float(ml1.z);
    const float M = fmaxf(fmaxf(m0, m1), fmaxf(m2, m3));
    const float e0 = __builtin_amdgcn_exp2f((m0 - M) * 1.4426950408889634f), e1 = __builtin_amdgcn_exp2f((m1 - M) * 1.4426950408889634f),
                e2 = __builtin_amdgcn_exp2f((m2 - M) * 1.4426950408889634f), e3 = __builtin_amdgcn_exp2f((m3 - M) * 1.4426950408889634f);
    const float Ls = (e0 * __uint_as_float(ml0.y) + e1 * __uint_as_float(ml0.w)) + (e2 * __uint_as_float(ml1.y) + e3 * __uint_as_float(ml1.w));
    const float inv = __builtin_amdgcn_rcpf(Ls);
    const int ob = L_OW + hd * 528 + (lane & 31) * 16;
    const f4 o = (*(LAS f4*)(L + ob) * e0 + *(LAS f4*)(L + ob + 8 * 528) * e1) + (*(LAS f4*)(L + ob + 16 * 528) * e2 + *(LAS f4*)(L + ob + 24 * 528) * e3);
    st8(rs, WS_OP + ((8 * g + hd) * 64 + m) * OPS + (lane & 31) * 8,
        v2u{nosent(pk_bf16(o[0] * inv, o[1] * inv)), nosent(pk_bf16(o[2] * inv, o[3] * inv))});
    if ((lane & 31) == 0) st4(rs, WS_OP + ((8 * g + hd) * 64 + m) * OPS + 256, nosent(__float_as_uint(fmaf(M, 1.4426950408889634f, __builtin_amdgcn_logf(Ls)))));
    STAMP(6);
  }
  if (w == 0) {
    unsigned lse = SENT;
    v4u v[16];
    gather<16, 4 * OPS, true>(ws, WS_OP + (h * 64 + g4) * OPS + n16 * 16 + 2 * OPS, v, WS_OP + (h * 64 + lane) * OPS + 256, lse);
    STAMP(7);
    const float lf = __uint_as_float(lse);
    const float M = wave_max(lf);
    const float e = __builtin_amdgcn_exp2f(lf - M);
    const float wt = e * __builtin_amdgcn_rcpf(wave_sum(e));
    const float hi = rbf(wt);
    *(LAS unsigned*)(L + L_WT + ((lane & 3) * 16 + (lane >> 2)) * 4) = pk_bf16(wt, wt);
    *(LAS unsigned*)(L + L_WT + 256 + ((lane & 3) * 16 + (lane >> 2)) * 4) = pk_bf16(wt - hi, wt - hi);
    LGKM0();
    const unsigned hm = (lane & 1) ? 0xffff0000u : 0x0000ffffu;
    const int dsel = (lane & 7) >> 1;
    const unsigned k0 = dsel == 0 ? hm : 0u, k1 = dsel == 1 ? hm : 0u, k2 = dsel == 2 ? hm : 0u, k3 = dsel == 3 ? hm : 0u;
    f4 acc = {0.f, 0.f, 0.f, 0.f};
#pragma unroll
    for (int i4 = 0; i4 < 4; ++i4) {
      const v4u t = *(LAS v4u*)(L + L_WT + ((lane >> 3) & 1) * 256 + g4 * 64 + i4 * 16);
#pragma unroll
      for (int k = 0; k < 4; ++k) {
        const v4u af = {t[k] & k0, t[k] & k1, t[k] & k2, t[k] & k3};
        acc = __builtin_amdgcn_mfma_f32_16x16x32_bf16(__builtin_bit_cast(b8, af), __builtin_bit_cast(b8, v[i4 * 4 + k]), acc, 0, 0, 0);
      }
    }
    float o[4];
#pragma unroll
    for (int k = 0; k < 4; ++k) o[k] = x32_sum(acc[k]);
    if (lane < 32) *(LAS v2u*)(L + L_OV + (8 * n16 + 4 * g4) * 2) = v2u{pk_bf16(o[0], o[1]), pk_bf16(o[2], o[3])};
    set_flag(L, F_OV, 1);
  } else {
    wait_flag(L, F_OV, 1);
  }
  STAMP(8);
  {
    f4 acc[2] = {f4{0.f, 0.f, 0.f, 0.f}, f4{0.f, 0.f, 0.f, 0.f}};
    wait_land(L, n_wo + 1); gemv_half<2>(L, wo_reg, L_OV, 2 * w, lane, acc);
    f4 fp = {0.f, 0.f, 0.f, 0.f};
    if (has_wd) {
      wait_flag(L, F_FFN, 2);
      fp = *(LAS f4*)(L + L_FP + ((2 * w + (n16 & 1)) * 16 + g4 * 4) * 4);
    }
    wait_land(L, n_wo + 2);
    STAMP(9);
    gemv_half<2>(L, wo_reg + 16384, L_OV + 128, 2 * w, lane, acc);
    const f4 r = (n16 == 0 ? acc[0] : acc[1]) + fp;
    if (n16 < 2) st16(rs, WS_FP + ((j8 * 4 + w) * 32 + h) * 128 + (n16 * 16 + g4 * 4) * 4, f4_pub(r));
    STAMP(10);
  }
  STAMP_OUT();
}

extern "C" void kernel_launch(void* const* d_in, const int* in_sizes, int n_in, void* d_out, int out_size, void* d_ws,
                              size_t ws_size, hipStream_t stream) {
  (void)in_sizes; (void)n_in; (void)out_size; (void)ws_size;
  (void)hipFuncSetAttribute((const void*)fused_decode, hipFuncAttributeMaxDynamicSharedMemorySize, LDS_BYTES);
  hipLaunchKernelGGL(fused_decode, dim3(256), dim3(512), LDS_BYTES, stream, (const u16*)d_in[0], (const u16*)d_in[1],
                     (const u16*)d_in[2], (const u16*)d_in[3], (const u16*)d_in[4], (const u16*)d_in[6], (const u16*)d_in[7],
                     (const u16*)d_in[8], (u16*)d_out, (unsigned char*)d_ws);
}
```

```cpp
#define PW 7
#define PI 9
#define PK 0
#define PC 0
#include <hip/hip_runtime.h>
#include <stdint.h>

#define STAMPS 0
#define WIN 12
#define SLP 1
#define GSLP 4
#define CPRIO 1
#define NT_AUX " nt"

typedef unsigned short u16;
typedef unsigned long long u64;
typedef unsigned v4u __attribute__((ext_vector_type(4)));
typedef unsigned v2u __attribute__((ext_vector_type(2)));
typedef float f4 __attribute__((ext_vector_type(4)));
typedef short s4 __attribute__((ext_vector_type(4)));
typedef __bf16 b8 __attribute__((ext_vector_type(8)));
typedef __bf16 b2 __attribute__((ext_vector_type(2)));
#define GAS __attribute__((address_space(1)))
#define LAS __attribute__((address_space(3)))
#define RLX_AGENT __ATOMIC_RELAXED, __HIP_MEMORY_SCOPE_AGENT
#define DEV __device__ __forceinline__

constexpr int SK = 32768, WKEYS = 4096, FF = 3584;
constexpr int WS_EPOCH = 0, WS_READY = 4096;
constexpr int WS_QP = WS_READY + 256 * 128;
constexpr int OPS = 384;
constexpr int WS_OP = WS_QP + 32 * 8 * 512;
constexpr int WS_GU = WS_OP + 32 * 64 * OPS;
constexpr int WS_FP = WS_GU + 28 * 8 * 512;
constexpr int WS_END = WS_FP + 32 * 32 * 128;
constexpr int WS_STAMP = 2 << 20;
constexpr unsigned SENT = 0xFFFFFFFFu;
constexpr int R0 = 0, R1 = 32768, R2 = 65536, R3 = 98304;
constexpr int L_XN = 131072;
constexpr int L_XR = L_XN + 2048;
constexpr int L_Q = L_XR + 2048;
constexpr int L_OW = L_Q + 8 * 288;
constexpr int L_ML = L_OW + 32 * 528;
constexpr int L_WT = L_ML + 256;
constexpr int L_OV = L_WT + 512;
constexpr int L_HV = L_OV + 256;
constexpr int L_FP = L_HV + 256;
constexpr int L_FLG = L_FP + 512;
constexpr int LDS_BYTES = L_FLG + 128;
static_assert(LDS_BYTES <= 160 * 1024, "lds");
enum { F_LAND0 = 0, F_LAND1 = 1, F_XN = 2, F_CLR = 3, F_GOK = 4, F_QC = 5, F_OC = 6, F_OV = 7, F_WQD = 8, F_GU0 = 9, F_HV = 10, F_FFN = 11, F_TAGLO = 12, F_TAGHI = 13, F_XR0 = 14, F_XR1 = 15 };

constexpr unsigned CODE_BYTES = 18432; constexpr int CODE_CHUNKS = (CODE_BYTES + 8191) / 8192;
constexpr unsigned SPIN_LDS = 1u << 22, SPIN_GLB = 1u << 18;

DEV float bf_lo(unsigned v) { return __uint_as_float(v << 16); }
DEV float bf_hi(unsigned v) { return __uint_as_float(v & 0xffff0000u); }
DEV unsigned pk_bf16(float lo, float hi) {
  b2 r; r[0] = (__bf16)lo; r[1] = (__bf16)hi; return __builtin_bit_cast(unsigned, r);
}
DEV float rbf(float x) { return __uint_as_float(pk_bf16(x, x) << 16); }
DEV unsigned nosent(unsigned v) { return v < 0xFFFFFFFEu ? v : 0xFFFFFFFEu; }
template <int CTRL> DEV float dpp(float x) {
  return __builtin_bit_cast(float, __builtin_amdgcn_mov_dpp(__builtin_bit_cast(int, x), CTRL, 0xf, 0xf, true));
}
constexpr int XOR1 = 0xB1, XOR2 = 0x4E, HMIR = 0x141, ROR8 = 0x128, ROR4 = 0x124;
DEV float x16_sum(float x) {
  auto s = __builtin_amdgcn_permlane16_swap(__float_as_uint(x), __float_as_uint(x), false, false);
  return __uint_as_float(s[0]) + __uint_as_float(s[1]);
}
DEV float x32_sum(float x) {
  auto s = __builtin_amdgcn_permlane32_swap(__float_as_uint(x), __float_as_uint(x), false, false);
  return __uint_as_float(s[0]) + __uint_as_float(s[1]);
}
DEV float x16_max(float x) {
  auto s = __builtin_amdgcn_permlane16_swap(__float_as_uint(x), __float_as_uint(x), false, false);
  return fmaxf(__uint_as_float(s[0]), __uint_as_float(s[1]));
}
DEV float x32_max(float x) {
  auto s = __builtin_amdgcn_permlane32_swap(__float_as_uint(x), __float_as_uint(x), false, false);
  return fmaxf(__uint_as_float(s[0]), __uint_as_float(s[1]));
}
DEV float wave_sum(float x) {
  x += dpp<XOR1>(x); x += dpp<XOR2>(x); x += dpp<HMIR>(x); x += dpp<ROR8>(x);
  return x32_sum(x16_sum(x));
}
DEV float wave_max(float x) {
  x = fmaxf(x, dpp<XOR1>(x)); x = fmaxf(x, dpp<XOR2>(x)); x = fmaxf(x, dpp<HMIR>(x)); x = fmaxf(x, dpp<ROR8>(x));
  return x32_max(x16_max(x));
}
#define CFENCE() asm volatile("" ::: "memory")
#define BARRIER() asm volatile("s_barrier" ::: "memory")
#define LGKM0() asm volatile("s_waitcnt lgkmcnt(0)" ::: "memory")
template <int N> DEV void waitvm() { asm volatile("s_waitcnt vmcnt(%0)" ::"n"(N) : "memory"); }

typedef LAS unsigned char* lptr;
DEV volatile LAS unsigned* flg(lptr L, int i) { return (volatile LAS unsigned*)(L + L_FLG) + i; }
DEV void wait_flag(lptr L, int i, unsigned v) {
  for (unsigned n = 0; n < SPIN_LDS; ++n) {
    if ((unsigned)__builtin_amdgcn_readfirstlane(*flg(L, i)) >= v) break;
    __builtin_amdgcn_s_sleep(SLP);
  }
  CFENCE();
}
DEV void wait_land(lptr L, unsigned v, int pair = 0) {
  for (unsigned n = 0; n < SPIN_LDS; ++n) {
    const v2u f = *(volatile LAS v2u*)(L + L_FLG + pair * 4);
    if ((unsigned)__builtin_amdgcn_readfirstlane(f.x < f.y ? f.x : f.y) >= v) break;
    __builtin_amdgcn_s_sleep(SLP);
  }
  CFENCE();
}
DEV void set_flag(lptr L, int i, unsigned v) { LGKM0(); *flg(L, i) = v; }
DEV void inc_flag(lptr L, int i, int lane) {
  LGKM0();
  if (lane == 0) __hip_atomic_fetch_add((LAS unsigned*)(L + L_FLG) + i, 1u, __ATOMIC_RELAXED, __HIP_MEMORY_SCOPE_WORKGROUP);
}
typedef __amdgpu_buffer_rsrc_t rsrc_t;
DEV v4u ld16(rsrc_t rs, int off) { return __builtin_amdgcn_raw_buffer_load_b128(rs, off, 0, 16); }
DEV v2u ld8(rsrc_t rs, int off) { return __builtin_amdgcn_raw_buffer_load_b64(rs, off, 0, 16); }
DEV unsigned ld4(rsrc_t rs, int off) { return __builtin_amdgcn_raw_buffer_load_b32(rs, off, 0, 16); }
DEV void st16(rsrc_t rs, int off, v4u v) { __builtin_amdgcn_raw_buffer_store_b128(v, rs, off, 0, 16); }
DEV void st8(rsrc_t rs, int off, v2u v) { __builtin_amdgcn_raw_buffer_store_b64(v, rs, off, 0, 16); }
DEV void st4(rsrc_t rs, int off, unsigned v) { __builtin_amdgcn_raw_buffer_store_b32(v, rs, off, 0, 16); }
DEV v4u f4_pub(f4 a) {
  return v4u{nosent(__float_as_uint(a[0])), nosent(__float_as_uint(a[1])), nosent(__float_as_uint(a[2])), nosent(__float_as_uint(a[3]))};
}
DEV f4 as_f4(v4u v) { return __builtin_bit_cast(f4, v); }

DEV unsigned max4(v4u v) { const unsigned a = v.x > v.y ? v.x : v.y, b = v.z > v.w ? v.z : v.w; return a > b ? a : b; }
DEV unsigned umax(unsigned a, unsigned b) { return a > b ? a : b; }
template <int S> DEV void ldx4(const unsigned char* ws, unsigned off, v4u& a, v4u& b, v4u& c, v4u& d) {
  constexpr int B = S > 1024 ? -S / 2 : 0;
  asm volatile("global_load_dwordx4 %0, %4, %5 offset:%9 sc1\n\tglobal_load_dwordx4 %1, %4, %5 offset:%6 sc1\n\t"
               "global_load_dwordx4 %2, %4, %5 offset:%7 sc1\n\tglobal_load_dwordx4 %3, %4, %5 offset:%8 sc1"
               : "+v"(a), "+v"(b), "+v"(c), "+v"(d) : "v"(off), "s"(ws), "n"(B + S), "n"(B + 2 * S), "n"(B + 3 * S), "n"(B) : "memory");
}
#define VM0_4(v, o) asm volatile("s_waitcnt vmcnt(0)" : "+v"(v[o]), "+v"(v[o + 1]), "+v"(v[o + 2]), "+v"(v[o + 3]) :: "memory")
#define PIN_4(v, o) asm volatile("" : "+v"(v[o]), "+v"(v[o + 1]), "+v"(v[o + 2]), "+v"(v[o + 3]) :: "memory")
template <int N, int S, bool X> DEV void gather(const unsigned char* ws, unsigned off, v4u (&v)[N], unsigned xoff, unsigned& xv) {
#pragma unroll
  for (int i = 0; i < N; ++i) v[i] = v4u{SENT, SENT, SENT, SENT};
  bool bad = true;
  for (unsigned n = 0; n < SPIN_GLB; ++n) {
    if (bad) {
      if (X) asm volatile("global_load_dword %0, %1, %2 sc1" : "+v"(xv) : "v"(xoff), "s"(ws) : "memory");
#pragma unroll
      for (int i = 0; i < N; i += 4) ldx4<S>(ws, off + i * S, v[i], v[i + 1], v[i + 2], v[i + 3]);
      VM0_4(v, 0);
#pragma unroll
      for (int i = 4; i < N; i += 4) PIN_4(v, i);
      if (X) asm volatile("" : "+v"(xv));
      unsigned mx = X ? xv : 0u;
#pragma unroll
      for (int i = 0; i < N; ++i) mx = umax(mx, max4(v[i]));
      bad = mx == SENT;
    }
    if (!__any(bad)) break;
    __builtin_amdgcn_s_sleep(GSLP);
  }
}

template <int NCB> DEV void gemv_half(lptr L, int tb, int xb, int c0, int lane, f4 (&acc)[NCB]) {
  const int g = lane >> 4, q = (lane >> 2) & 3, p = lane & 3;
  const int rr = 4 * (g & 1) + q;
  const int base = tb + 256 * (4 * g + q) + 8 * p;
#pragma unroll
  for (int ks = 0; ks < 2; ++ks) {
    const v2u x0 = *(LAS v2u*)(L + xb + 64 * ks + 8 * g), x1 = *(LAS v2u*)(L + xb + 64 * ks + 32 + 8 * g);
    const v4u xf = {x0.x, x0.y, x1.x, x1.y};
#pragma unroll
    for (int i = 0; i < NCB; ++i) {
      const int a = base + (((c0 + i) ^ rr) << 5) + 8192 * ks;
      const v2u t0 = __builtin_bit_cast(v2u, __builtin_amdgcn_ds_read_tr16_b64_v4i16((LAS s4*)(L + a)));
      const v2u t1 = __builtin_bit_cast(v2u, __builtin_amdgcn_ds_read_tr16_b64_v4i16((LAS s4*)(L + a + 4096)));
      const v4u wf = {t0.x, t0.y, t1.x, t1.y};
      acc[i] = __builtin_amdgcn_mfma_f32_16x16x32_bf16(__builtin_bit_cast(b8, wf), __builtin_bit_cast(b8, xf), acc[i], 0, 0, 0);
    }
  }
}

template <int STRIDE, bool WAIT> DEV void issue_half(lptr L, int j, const void* base, unsigned vbase, unsigned r4, unsigned lds, int pub) {
  unsigned voff = vbase + r4 * STRIDE + j * 4 * STRIDE;
  unsigned m = (unsigned)(size_t)L + lds + j * 1024;
#pragma unroll
  for (int k = 0; k < 8; ++k) {
    asm volatile("s_mov_b32 m0, %0\n\ts_nop 0\n\tglobal_load_lds_dwordx4 %1, %2" NT_AUX ::"s"(m), "v"(voff), "s"(base) : "memory");
    voff += 8 * STRIDE; m += 2048;
    if (WAIT) waitvm<WIN - 1>();
    if (k == (WIN - 2) % 8 && pub > 0) *flg(L, j) = (unsigned)pub;
  }
}

#define STAMP(i) do { if (i == 0) t_begin = __builtin_amdgcn_s_memrealtime(); else if (i == PI && wave == PW) { *(volatile LAS u64*)(L + L_FLG + 64) = __builtin_amdgcn_s_memrealtime(); } } while (0)
#define STAMP_OUT()

__global__ void __launch_bounds__(512, 2)
fused_decode(const u16* __restrict__ x, const u16* __restrict__ kc, const u16* __restrict__ vc, const u16* __restrict__ wq,
             const u16* __restrict__ wo, const u16* __restrict__ wg, const u16* __restrict__ wu, const u16* __restrict__ wd,
             u16* __restrict__ out, unsigned char* ws) {
  extern __shared__ __attribute__((aligned(16))) unsigned char smem[];
  lptr L = (lptr)smem;
  const int c = blockIdx.x, tid = threadIdx.x, lane = tid & 63, wave = __builtin_amdgcn_readfirstlane(tid >> 6);
  u64 t_begin;
  STAMP(0);
  u64 pc0; asm volatile("s_getpc_b64 %0" : "=s"(pc0));
  const int g = (c & 7) >> 1, m = (c & 1) * 32 + (c >> 3), hh = m >> 3, j8 = m & 7, h = 8 * g + hh;
  const bool has_gu = hh != 0, has_wd = hh != 7;
  const unsigned n_gu = 4, n_wd = has_gu ? 8 : 4, n_wo = n_wd + (has_wd ? 2 : 0), n_all = n_wo + 2;
  const int wo_reg = has_wd ? R2 : R0;
  constexpr int LAG = (WIN - 2) / 8 + 1;

  if (__builtin_expect(wave < 2, 1)) {
    const int j = wave;
    const unsigned r4 = lane >> 4;
    const unsigned sw = ((lane & 15) ^ ((4 * j + r4) << 1)) << 4;
    const char* bq = (const char*)wq + (size_t)j8 * 128 * 8192 + h * 256;
    __builtin_amdgcn_s_setprio(3);
    asm volatile("s_mov_b32 m0, %0\n\ts_nop 0\n\tglobal_load_lds_dwordx4 %1, %2" ::"s"((unsigned)(size_t)L + L_XR + j * 1024), "v"(lane * 16 + j * 1024), "s"(x) : "memory");
    issue_half<8192, false>(L, j, bq, sw, r4, R0, 0);
    issue_half<8192, false>(L, j, bq, sw + 64 * 8192, r4, R0 + 16384, 0);
    BARRIER();
    waitvm<16>(); *flg(L, F_XR0 + j) = 1u;
    const size_t kvo = ((size_t)g * SK + (SK - WKEYS) + m * 64) * 256;
    int i = 2;
    issue_half<256, true>(L, j, (const char*)kc + kvo, sw, r4, R1, i - LAG + 1); ++i;
    STAMP(1);
    issue_half<256, true>(L, j, (const char*)vc + kvo, sw, r4, R1 + 16384, i - LAG + 1); ++i;
    STAMP(2);
    if (has_gu) {
      const int cbg = 7 * g + hh - 1, rbg = j8 & 3;
      const char* bg = (const char*)((j8 & 4) ? wu : wg) + (size_t)rbg * 256 * 7168 + cbg * 256;
      issue_half<7168, true>(L, j, bg, sw, r4, R2, i - LAG + 1); ++i;
      issue_half<7168, true>(L, j, bg, sw + 64 * 7168, r4, R2 + 16384, i - LAG + 1); ++i;
      issue_half<7168, true>(L, j, bg, sw + 128 * 7168, r4, R3, i - LAG + 1); ++i;
      issue_half<7168, true>(L, j, bg, sw + 192 * 7168, r4, R3 + 16384, i - LAG + 1); ++i;
    }
    STAMP(3);
    wait_flag(L, F_WQD, 4);
    if (has_wd) {
      const char* bd = (const char*)wd + (size_t)(7 * g + hh) * 128 * 2048 + j8 * 256;
      issue_half<2048, true>(L, j, bd, sw, r4, R0, i - LAG + 1); ++i;
      issue_half<2048, true>(L, j, bd, sw + 64 * 2048, r4, R0 + 16384, i - LAG + 1); ++i;
      if (has_gu) wait_flag(L, F_GU0, 2);
    }
    STAMP(4);
    const char* bo = (const char*)wo + (size_t)h * 128 * 2048 + j8 * 256;
    issue_half<2048, true>(L, j, bo, sw, r4, wo_reg, i - LAG + 1); ++i;
    issue_half<2048, true>(L, j, bo, sw + 64 * 2048, r4, wo_reg + 16384, i - LAG + 1); ++i;
    STAMP(5);
    if (WIN > 17) { waitvm<16>(); *flg(L, j) = n_all - 2; }
    waitvm<8>(); *flg(L, j) = n_all - 1;
    waitvm<0>(); *flg(L, j) = n_all;
    STAMP(6);
    STAMP_OUT();
    return;
  }

  const rsrc_t rs = __builtin_amdgcn_make_buffer_rsrc(ws, 0, WS_END, 0x00020000);
  if (wave == 7) {
    if (lane < 32) *flg(L, lane) = 0u;
    LGKM0();
  }
  unsigned pf0 = 0;
  if (wave < 2 + CODE_CHUNKS) {
    const unsigned o = (wave - 2) * 8192 + lane * 128;
    pf0 = *(const GAS unsigned*)((pc0 & ~127ull) + (o < CODE_BYTES ? o : 0));
  }

  if (wave >= 6) {
    const int f = wave - 6;
    f4 xres = {0.f, 0.f, 0.f, 0.f};
    if (f == 0) {
      u64 tag;
      asm volatile("s_load_dwordx2 %0, %1, 0x0\n\ts_waitcnt lgkmcnt(0)" : "=s"(tag) : "s"(ws) : "memory");
      tag += 1;
      const v4u sv = {SENT, SENT, SENT, SENT};
      const int l5 = lane & 31;
      st16(rs, lane < 32 ? WS_QP + (h * 8 + j8) * 512 + l5 * 16 : WS_FP + ((j8 * 4 + (l5 >> 3)) * 32 + h) * 128 + (l5 & 7) * 16, sv);
      st16(rs, WS_OP + ((8 * g + (lane >> 4)) * 64 + m) * OPS + (lane & 15) * 16, sv);
      st16(rs, WS_OP + ((8 * g + 4 + (lane >> 4)) * 64 + m) * OPS + (lane & 15) * 16, sv);
      if (lane < 8) st4(rs, WS_OP + ((8 * g + lane) * 64 + m) * OPS + 256, SENT);
      if (has_gu && lane < 32) st16(rs, WS_GU + ((7 * g + hh - 1) * 8 + j8) * 512 + lane * 16, sv);
      BARRIER();
      waitvm<0>();
      if (lane == 0) __hip_atomic_store((GAS u64*)(ws + WS_READY) + (g * 64 + m) * 16, tag, RLX_AGENT);
      *flg(L, F_TAGLO) = (unsigned)tag; *flg(L, F_TAGHI) = (unsigned)(tag >> 32);
      set_flag(L, F_CLR, 1);
      STAMP(1);
      wait_land(L, 1);
      {
        bool bad = true;
        for (unsigned n = 0; n < SPIN_GLB; ++n) {
          if (bad) bad = __hip_atomic_load((GAS u64*)(ws + WS_READY) + (g * 64 + lane) * 16, RLX_AGENT) != tag;
          if (!__any(bad)) break;
          __builtin_amdgcn_s_sleep(GSLP);
        }
      }
      set_flag(L, F_GOK, 1);
      STAMP(2);
    } else {
      BARRIER();
      wait_land(L, 1, F_XR0);
      STAMP(2);
      const v4u xa = *(LAS v4u*)(L + L_XR + lane * 32), xb = *(LAS v4u*)(L + L_XR + lane * 32 + 16);
      {
        const v2u xr = *(LAS v2u*)(L + L_XR + (j8 * 128 + g * 32 + (lane & 7) * 4) * 2);
        xres = f4{bf_lo(xr.x), bf_hi(xr.x), bf_lo(xr.y), bf_hi(xr.y)};
      }
      float v[16];
#pragma unroll
      for (int k = 0; k < 4; ++k) { v[2 * k] = bf_lo(xa[k]); v[2 * k + 1] = bf_hi(xa[k]); v[8 + 2 * k] = bf_lo(xb[k]); v[9 + 2 * k] = bf_hi(xb[k]); }
      float s = 0.f;
#pragma unroll
      for (int k = 0; k < 16; ++k) s += v[k];
      const float mean = wave_sum(s) * (1.f / 1024.f);
      float q2 = 0.f;
#pragma unroll
      for (int k = 0; k < 16; ++k) { const float d = v[k] - mean; q2 = fmaf(d, d, q2); }
      const float var = wave_sum(q2) * (1.f / 1024.f);
      const float mb = rbf(mean), rden = 1.0f / rbf(__builtin_sqrtf(rbf(rbf(var) + 1e-5f)));
      v4u o0, o1;
#pragma unroll
      for (int k = 0; k < 4; ++k) {
        o0[k] = pk_bf16(rbf(v[2 * k] - mb) * rden, rbf(v[2 * k + 1] - mb) * rden);
        o1[k] = pk_bf16(rbf(v[8 + 2 * k] - mb) * rden, rbf(v[9 + 2 * k] - mb) * rden);
      }
      *(LAS v4u*)(L + L_XN + lane * 32) = o0;
      *(LAS v4u*)(L + L_XN + lane * 32 + 16) = o1;
      set_flag(L, F_XN, 1);
      STAMP(1);
    }
    if (has_gu) {
      wait_flag(L, F_XN, 1);
      f4 acc[4];
#pragma unroll
      for (int i = 0; i < 4; ++i) acc[i] = f4{0.f, 0.f, 0.f, 0.f};
      const int xb = L_XN + (j8 & 3) * 512;
      wait_land(L, n_gu + 1); gemv_half<4>(L, R2, xb, 4 * f, lane, acc);
      wait_land(L, n_gu + 2); gemv_half<4>(L, R2 + 16384, xb + 128, 4 * f, lane, acc);
      asm volatile("" :: "v"(acc[0]), "v"(acc[1]), "v"(acc[2]), "v"(acc[3]));
      inc_flag(L, F_GU0, lane);
      STAMP(3);
      wait_land(L, n_gu + 3); gemv_half<4>(L, R3, xb + 256, 4 * f, lane, acc);
      wait_land(L, n_gu + 4); gemv_half<4>(L, R3 + 16384, xb + 384, 4 * f, lane, acc);
      const int n = lane & 15;
      const f4 r = n == 0 ? acc[0] : n == 1 ? acc[1] : n == 2 ? acc[2] : acc[3];
      wait_flag(L, F_CLR, 1);
      if (n < 4) st16(rs, WS_GU + ((7 * g + hh - 1) * 8 + j8) * 512 + ((4 * f + n) * 16 + (lane >> 4) * 4) * 4, f4_pub(r));
      STAMP(4);
    }
    if (has_wd) {
      if (f == 0) {
        v4u v[4];
        unsigned dummy = 0; gather<4, 512, false>(ws, WS_GU + ((7 * g + hh) * 8 + (lane >> 5) * 4) * 512 + (lane & 31) * 16, v, 0, dummy);
        STAMP(5);
        const f4 s = (as_f4(v[0]) + as_f4(v[1])) + (as_f4(v[2]) + as_f4(v[3]));
        float hv[4];
#pragma unroll
        for (int k = 0; k < 4; ++k) {
          auto sw = __builtin_amdgcn_permlane32_swap(__float_as_uint(s[k]), __float_as_uint(s[k]), false, false);
          const float gt = rbf(__uint_as_float(sw[0])), up = rbf(__uint_as_float(sw[1]));
          const float sg = rbf(gt * __builtin_amdgcn_rcpf(1.f + __expf(-gt)));
          hv[k] = sg * up;
        }
        if (lane < 32) *(LAS v2u*)(L + L_HV + lane * 8) = v2u{pk_bf16(hv[0], hv[1]), pk_bf16(hv[2], hv[3])};
        set_flag(L, F_HV, 1);
      } else {
        wait_flag(L, F_HV, 1);
      }
      f4 acc[4];
#pragma unroll
      for (int i = 0; i < 4; ++i) acc[i] = f4{0.f, 0.f, 0.f, 0.f};
      wait_land(L, n_wd + 1); gemv_half<4>(L, R0, L_HV, 4 * f, lane, acc);
      wait_land(L, n_wd + 2); gemv_half<4>(L, R0 + 16384, L_HV + 128, 4 * f, lane, acc);
      const int n = lane & 15;
      const f4 r = n == 0 ? acc[0] : n == 1 ? acc[1] : n == 2 ? acc[2] : acc[3];
      if (n < 4) *(LAS f4*)(L + L_FP + ((4 * f + n) * 16 + (lane >> 4) * 4) * 4) = r;
      inc_flag(L, F_FFN, lane);
      STAMP(6);
    }
    if (hh == 0 && f == 1) {
      wait_flag(L, F_CLR, 1);
      const u64 tag = (u64)*flg(L, F_TAGLO) | ((u64)*flg(L, F_TAGHI) << 32);
      for (unsigned n = 0; n < SPIN_GLB; ++n) {
        bool ok = true;
#pragma unroll
        for (int i = 0; i < 4; ++i) ok &= __hip_atomic_load((GAS u64*)(ws + WS_READY) + (i * 64 + lane) * 16, RLX_AGENT) == tag;
        if (__all(ok)) break;
        __builtin_amdgcn_s_sleep(GSLP);
      }
      CFENCE();
      if (c == 0 && lane == 0) __hip_atomic_store((GAS u64*)(ws + WS_EPOCH), tag, RLX_AGENT);
      STAMP(7);
      v4u v[4];
      unsigned dummy = 0; gather<4, 1024, false>(ws, WS_FP + (j8 * 4 + g) * 4096 + lane * 16, v, 0, dummy);
      STAMP(8);
      f4 s = (as_f4(v[0]) + as_f4(v[1])) + (as_f4(v[2]) + as_f4(v[3]));
#pragma unroll
      for (int k = 0; k < 4; ++k) { float t = s[k]; t += dpp<ROR8>(t); s[k] = x32_sum(x16_sum(t)) + xres[k]; }
      if (lane < 8) *(GAS v2u*)(out + j8 * 128 + g * 32 + lane * 4) = v2u{pk_bf16(s[0], s[1]), pk_bf16(s[2], s[3])};
      STAMP(9);
      if (c == PC) {
        LGKM0();
        const u64 tp = *(volatile LAS u64*)(L + L_FLG + 64);
        const u64 now = __builtin_amdgcn_s_memrealtime();
        u64 d = (tp - t_begin) * PK; if (d > 20000) d = 20000;
        while (__builtin_amdgcn_s_memrealtime() < now + d) __builtin_amdgcn_s_sleep(1);
      }
    }
    STAMP_OUT();
    return;
  }

  const int w = wave - 2, n16 = lane & 15, g4 = lane >> 4;
  BARRIER();
  asm volatile("" :: "v"(pf0));
  __builtin_amdgcn_s_setprio(CPRIO);
  {
    f4 acc[2] = {f4{0.f, 0.f, 0.f, 0.f}, f4{0.f, 0.f, 0.f, 0.f}};
    wait_flag(L, F_XN, 1);
    wait_land(L, 1); gemv_half<2>(L, R0, L_XN + j8 * 256, 2 * w, lane, acc);
    wait_flag(L, F_CLR, 1);
    wait_land(L, 2);
    STAMP(1);
    gemv_half<2>(L, R0 + 16384, L_XN + j8 * 256 + 128, 2 * w, lane, acc);
    const f4 r = n16 == 0 ? acc[0] : acc[1];
    if (n16 < 2) st16(rs, WS_QP + (h * 8 + j8) * 512 + ((2 * w + n16) * 16 + g4 * 4) * 4, f4_pub(r));
    inc_flag(L, F_WQD, lane);
    STAMP(2);
  }
  {
    wait_flag(L, F_GOK, 1);
    v4u v[8];
    unsigned dummy = 0; gather<8, 1024, false>(ws, WS_QP + (8 * g + 2 * w) * 4096 + lane * 16, v, 0, dummy);
    STAMP(3);
    const f4 a = (as_f4(v[0]) + as_f4(v[1])) + (as_f4(v[2]) + as_f4(v[3]));
    const f4 b = (as_f4(v[4]) + as_f4(v[5])) + (as_f4(v[6]) + as_f4(v[7]));
    float qv[4];
#pragma unroll
    for (int k = 0; k < 4; ++k) {
      auto sw = __builtin_amdgcn_permlane32_swap(__float_as_uint(a[k]), __float_as_uint(b[k]), false, false);
      qv[k] = __uint_as_float(sw[0]) + __uint_as_float(sw[1]);
    }
    *(LAS v2u*)(L + L_Q + (2 * w + (lane >> 5)) * 288 + (lane & 31) * 8) = v2u{pk_bf16(qv[0], qv[1]), pk_bf16(qv[2], qv[3])};
    inc_flag(L, F_QC, lane);
  }
  {
    wait_land(L, 3);
    v4u kf[4];
    const int krow = 16 * w + n16;
#pragma unroll
    for (int ks = 0; ks < 4; ++ks) kf[ks] = *(LAS v4u*)(L + R1 + krow * 256 + (((4 * ks + g4) ^ ((n16 & 7) << 1)) << 4));
    wait_flag(L, F_QC, 4);
    STAMP(4);
    f4 s = {0.f, 0.f, 0.f, 0.f};
#pragma unroll
    for (int ks = 0; ks < 4; ++ks) {
      const v4u qf = *(LAS v4u*)(L + L_Q + (n16 & 7) * 288 + ks * 64 + g4 * 16);
      s = __builtin_amdgcn_mfma_f32_16x16x32_bf16(__builtin_bit_cast(b8, kf[ks]), __builtin_bit_cast(b8, qf), s, 0, 0, 0);
    }
    float sc[4];
#pragma unroll
    for (int r = 0; r < 4; ++r) sc[r] = rbf(rbf(s[r]) * 0.08838834764831845f);
    const float mw = x32_max(x16_max(fmaxf(fmaxf(sc[0], sc[1]), fmaxf(sc[2], sc[3]))));
    float p[4];
#pragma unroll
    for (int r = 0; r < 4; ++r) p[r] = __builtin_amdgcn_exp2f((sc[r] - mw) * 1.4426950408889634f);
    const float lw = x32_sum(x16_sum((p[0] + p[1]) + (p[2] + p[3])));
    const v2u pf = {pk_bf16(p[0], p[1]), pk_bf16(p[2], p[3])};
    wait_land(L, 4);
    const int q = (lane >> 2) & 3, pp = lane & 3, rr = 4 * (g4 & 1) + q;
    const int vb = R1 + 16384 + (16 * w + 4 * g4 + q) * 256 + 8 * pp;
    if (lane < 8) *(LAS v2u*)(L + L_ML + (lane * 4 + w) * 8) = v2u{__float_as_uint(mw), __float_as_uint(lw)};
#pragma unroll
    for (int db = 0; db < 8; ++db) {
      const s4 vf = __builtin_amdgcn_ds_read_tr16_b64_v4i16((LAS s4*)(L + vb + ((db ^ rr) << 5)));
      const f4 o = __builtin_amdgcn_mfma_f32_16x16x16bf16_1k(vf, __builtin_bit_cast(s4, pf), f4{0.f, 0.f, 0.f, 0.f}, 0, 0, 0);
      if (n16 < 8) *(LAS f4*)(L + L_OW + (w * 8 + n16) * 528 + (db * 16 + g4 * 4) * 4) = o;
    }
    inc_flag(L, F_OC, lane);
    wait_flag(L, F_OC, 4);
    STAMP(5);
    const int hd = 2 * w + (lane >> 5);
    const v4u ml0 = *(LAS v4u*)(L + L_ML + hd * 32), ml1 = *(LAS v4u*)(L + L_ML + hd * 32 + 16);
    const float m0 = __uint_as_float(ml0.x), m1 = __uint_as_float(ml0.z), m2 = __uint_as_float(ml1.x), m3 = __uint_as_float(ml1.z);
    const float M = fmaxf(fmaxf(m0, m1), fmaxf(m2, m3));
    const float e0 = __builtin_amdgcn_exp2f((m0 - M) * 1.4426950408889634f), e1 = __builtin_amdgcn_exp2f((m1 - M) * 1.4426950408889634f),
                e2 = __builtin_amdgcn_exp2f((m2 - M) * 1.4426950408889634f), e3 = __builtin_amdgcn_exp2f((m3 - M) * 1.4426950408889634f);
    const float Ls = (e0 * __uint_as_float(ml0.y) + e1 * __uint_as_float(ml0.w)) + (e2 * __uint_as_float(ml1.y) + e3 * __uint_as_float(ml1.w));
    const float inv = __builtin_amdgcn_rcpf(Ls);
    const int ob = L_OW + hd * 528 + (lane & 31) * 16;
    const f4 o = (*(LAS f4*)(L + ob) * e0 + *(LAS f4*)(L + ob + 8 * 528) * e1) + (*(LAS f4*)(L + ob + 16 * 528) * e2 + *(LAS f4*)(L + ob + 24 * 528) * e3);
    st8(rs, WS_OP + ((8 * g + hd) * 64 + m) * OPS + (lane & 31) * 8,
        v2u{nosent(pk_bf16(o[0] * inv, o[1] * inv)), nosent(pk_bf16(o[2] * inv, o[3] * inv))});
    if ((lane & 31) == 0) st4(rs, WS_OP + ((8 * g + hd) * 64 + m) * OPS + 256, nosent(__float_as_uint(fmaf(M, 1.4426950408889634f, __builtin_amdgcn_logf(Ls)))));
    STAMP(6);
  }
  if (w == 0) {
    unsigned lse = SENT;
    v4u v[16];
    gather<16, 4 * OPS, true>(ws, WS_OP + (h * 64 + g4) * OPS + n16 * 16 + 2 * OPS, v, WS_OP + (h * 64 + lane) * OPS + 256, lse);
    STAMP(7);
    const float lf = __uint_as_float(lse);
    const float M = wave_max(lf);
    const float e = __builtin_amdgcn_exp2f(lf - M);
    const float wt = e * __builtin_amdgcn_rcpf(wave_sum(e));
    const float hi = rbf(wt);
    *(LAS unsigned*)(L + L_WT + ((lane & 3) * 16 + (lane >> 2)) * 4) = pk_bf16(wt, wt);
    *(LAS unsigned*)(L + L_WT + 256 + ((lane & 3) * 16 + (lane >> 2)) * 4) = pk_bf16(wt - hi, wt - hi);
    LGKM0();
    const unsigned hm = (lane & 1) ? 0xffff0000u : 0x0000ffffu;
    const int dsel = (lane & 7) >> 1;
    const unsigned k0 = dsel == 0 ? hm : 0u, k1 = dsel == 1 ? hm : 0u, k2 = dsel == 2 ? hm : 0u, k3 = dsel == 3 ? hm : 0u;
    f4 acc = {0.f, 0.f, 0.f, 0.f};
#pragma unroll
    for (int i4 = 0; i4 < 4; ++i4) {
      const v4u t = *(LAS v4u*)(L + L_WT + ((lane >> 3) & 1) * 256 + g4 * 64 + i4 * 16);
#pragma unroll
      for (int k = 0; k < 4; ++k) {
        const v4u af = {t[k] & k0, t[k] & k1, t[k] & k2, t[k] & k3};
        acc = __builtin_amdgcn_mfma_f32_16x16x32_bf16(__builtin_bit_cast(b8, af), __builtin_bit_cast(b8, v[i4 * 4 + k]), acc, 0, 0, 0);
      }
    }
    float o[4];
#pragma unroll
    for (int k = 0; k < 4; ++k) o[k] = x32_sum(acc[k]);
    if (lane < 32) *(LAS v2u*)(L + L_OV + (8 * n16 + 4 * g4) * 2) = v2u{pk_bf16(o[0], o[1]), pk_bf16(o[2], o[3])};
    set_flag(L, F_OV, 1);
  } else {
    wait_flag(L, F_OV, 1);
  }
  STAMP(8);
  {
    f4 acc[2] = {f4{0.f, 0.f, 0.f, 0.f}, f4{0.f, 0.f, 0.f, 0.f}};
    wait_land(L, n_wo + 1); gemv_half<2>(L, wo_reg, L_OV, 2 * w, lane, acc);
    f4 fp = {0.f, 0.f, 0.f, 0.f};
    if (has_wd) {
      wait_flag(L, F_FFN, 2);
      fp = *(LAS f4*)(L + L_FP + ((2 * w + (n16 & 1)) * 16 + g4 * 4) * 4);
    }
    wait_land(L, n_wo + 2);
    STAMP(9);
    gemv_half<2>(L, wo_reg + 16384, L_OV + 128, 2 * w, lane, acc);
    const f4 r = (n16 == 0 ? acc[0] : acc[1]) + fp;
    if (n16 < 2) st16(rs, WS_FP + ((j8 * 4 + w) * 32 + h) * 128 + (n16 * 16 + g4 * 4) * 4, f4_pub(r));
    STAMP(10);
  }
  STAMP_OUT();
}

extern "C" void kernel_launch(void* const* d_in, const int* in_sizes, int n_in, void* d_out, int out_size, void* d_ws,
                              size_t ws_size, hipStream_t stream) {
  (void)in_sizes; (void)n_in; (void)out_size; (void)ws_size;
  (void)hipFuncSetAttribute((const void*)fused_decode, hipFuncAttributeMaxDynamicSharedMemorySize, LDS_BYTES);
  hipLaunchKernelGGL(fused_decode, dim3(256), dim3(512), LDS_BYTES, stream, (const u16*)d_in[0], (const u16*)d_in[1],
                     (const u16*)d_in[2], (const u16*)d_in[3], (const u16*)d_in[4], (const u16*)d_in[6], (const u16*)d_in[7],
                     (const u16*)d_in[8], (u16*)d_out, (unsigned char*)d_ws);
}
```

```cpp
#define PW 7
#define PI 9
#define PK 0
#define PC 0
#include <hip/hip_runtime.h>
#include <stdint.h>

#define STAMPS 0
#define WIN 12
#define SLP 1
#define GSLP 4
#define CPRIO 1
#define NT_AUX " nt"

typedef unsigned short u16;
typedef unsigned long long u64;
typedef unsigned v4u __attribute__((ext_vector_type(4)));
typedef unsigned v2u __attribute__((ext_vector_type(2)));
typedef float f4 __attribute__((ext_vector_type(4)));
typedef short s4 __attribute__((ext_vector_type(4)));
typedef __bf16 b8 __attribute__((ext_vector_type(8)));
typedef __bf16 b2 __attribute__((ext_vector_type(2)));
#define GAS __attribute__((address_space(1)))
#define LAS __attribute__((address_space(3)))
#define RLX_AGENT __ATOMIC_RELAXED, __HIP_MEMORY_SCOPE_AGENT
#define DEV __device__ __forceinline__

constexpr int SK = 32768, WKEYS = 4096, FF = 3584;
constexpr int WS_EPOCH = 0, WS_READY = 4096;
constexpr int WS_QP = WS_READY + 256 * 128;
constexpr int OPS = 384;
constexpr int WS_OP = WS_QP + 32 * 8 * 512;
constexpr int WS_GU = WS_OP + 32 * 64 * OPS;
constexpr int WS_FP = WS_GU + 28 * 8 * 512;
constexpr int WS_END = WS_FP + 32 * 32 * 128;
constexpr int WS_STAMP = 2 << 20;
constexpr unsigned SENT = 0xFFFFFFFFu;
constexpr int R0 = 0, R1 = 32768, R2 = 65536, R3 = 98304;
constexpr int L_XN = 131072;
constexpr int L_XR = L_XN + 2048;
constexpr int L_Q = L_XR + 2048;
constexpr int L_OW = L_Q + 8 * 288;
constexpr int L_ML = L_OW + 32 * 528;
constexpr int L_WT = L_ML + 256;
constexpr int L_OV = L_WT + 512;
constexpr int L_HV = L_OV + 256;
constexpr int L_FP = L_HV + 256;
constexpr int L_FLG = L_FP + 512;
constexpr int LDS_BYTES = L_FLG + 128;
static_assert(LDS_BYTES <= 160 * 1024, "lds");
enum { F_LAND0 = 0, F_LAND1 = 1, F_XN = 2, F_CLR = 3, F_GOK = 4, F_QC = 5, F_OC = 6, F_OV = 7, F_WQD = 8, F_GU0 = 9, F_HV = 10, F_FFN = 11, F_TAGLO = 12, F_TAGHI = 13, F_XR0 = 14, F_XR1 = 15 };

constexpr unsigned CODE_BYTES = 18432; constexpr int CODE_CHUNKS = (CODE_BYTES + 8191) / 8192;
constexpr unsigned SPIN_LDS = 1u << 22, SPIN_GLB = 1u << 18;

DEV float bf_lo(unsigned v) { return __uint_as_float(v << 16); }
DEV float bf_hi(unsigned v) { return __uint_as_float(v & 0xffff0000u); }
DEV unsigned pk_bf16(float lo, float hi) {
  b2 r; r[0] = (__bf16)lo; r[1] = (__bf16)hi; return __builtin_bit_cast(unsigned, r);
}
DEV float rbf(float x) { return __uint_as_float(pk_bf16(x, x) << 16); }
DEV unsigned nosent(unsigned v) { return v < 0xFFFFFFFEu ? v : 0xFFFFFFFEu; }
template <int CTRL> DEV float dpp(float x) {
  return __builtin_bit_cast(float, __builtin_amdgcn_mov_dpp(__builtin_bit_cast(int, x), CTRL, 0xf, 0xf, true));
}
constexpr int XOR1 = 0xB1, XOR2 = 0x4E, HMIR = 0x141, ROR8 = 0x128, ROR4 = 0x124;
DEV float x16_sum(float x) {
  auto s = __builtin_amdgcn_permlane16_swap(__float_as_uint(x), __float_as_uint(x), false, false);
  return __uint_as_float(s[0]) + __uint_as_float(s[1]);
}
DEV float x32_sum(float x) {
  auto s = __builtin_amdgcn_permlane32_swap(__float_as_uint(x), __float_as_uint(x), false, false);
  return __uint_as_float(s[0]) + __uint_as_float(s[1]);
}
DEV float x16_max(float x) {
  auto s = __builtin_amdgcn_permlane16_swap(__float_as_uint(x), __float_as_uint(x), false, false);
  return fmaxf(__uint_as_float(s[0]), __uint_as_float(s[1]));
}
DEV float x32_max(float x) {
  auto s = __builtin_amdgcn_permlane32_swap(__float_as_uint(x), __float_as_uint(x), false, false);
  return fmaxf(__uint_as_float(s[0]), __uint_as_float(s[1]));
}
DEV float wave_sum(float x) {
  x += dpp<XOR1>(x); x += dpp<XOR2>(x); x += dpp<HMIR>(x); x += dpp<ROR8>(x);
  return x32_sum(x16_sum(x));
}
DEV float wave_max(float x) {
  x = fmaxf(x, dpp<XOR1>(x)); x = fmaxf(x, dpp<XOR2>(x)); x = fmaxf(x, dpp<HMIR>(x)); x = fmaxf(x, dpp<ROR8>(x));
  return x32_max(x16_max(x));
}
#define CFENCE() asm volatile("" ::: "memory")
#define BARRIER() asm volatile("s_barrier" ::: "memory")
#define LGKM0() asm volatile("s_waitcnt lgkmcnt(0)" ::: "memory")
template <int N> DEV void waitvm() { asm volatile("s_waitcnt vmcnt(%0)" ::"n"(N) : "memory"); }

typedef LAS unsigned char* lptr;
DEV volatile LAS unsigned* flg(lptr L, int i) { return (volatile LAS unsigned*)(L + L_FLG) + i; }
DEV void wait_flag(lptr L, int i, unsigned v) {
  for (unsigned n = 0; n < SPIN_LDS; ++n) {
    if ((unsigned)__builtin_amdgcn_readfirstlane(*flg(L, i)) >= v) break;
    __builtin_amdgcn_s_sleep(SLP);
  }
  CFENCE();
}
DEV void wait_land(lptr L, unsigned v, int pair = 0) {
  for (unsigned n = 0; n < SPIN_LDS; ++n) {
    const v2u f = *(volatile LAS v2u*)(L + L_FLG + pair * 4);
    if ((unsigned)__builtin_amdgcn_readfirstlane(f.x < f.y ? f.x : f.y) >= v) break;
    __builtin_amdgcn_s_sleep(SLP);
  }
  CFENCE();
}
DEV void set_flag(lptr L, int i, unsigned v) { LGKM0(); *flg(L, i) = v; }
DEV void inc_flag(lptr L, int i, int lane) {
  LGKM0();
  if (lane == 0) __hip_atomic_fetch_add((LAS unsigned*)(L + L_FLG) + i, 1u, __ATOMIC_RELAXED, __HIP_MEMORY_SCOPE_WORKGROUP);
}
typedef __amdgpu_buffer_rsrc_t rsrc_t;
DEV v4u ld16(rsrc_t rs, int off) { return __builtin_amdgcn_raw_buffer_load_b128(rs, off, 0, 16); }
DEV v2u ld8(rsrc_t rs, int off) { return __builtin_amdgcn_raw_buffer_load_b64(rs, off, 0, 16); }
DEV unsigned ld4(rsrc_t rs, int off) { return __builtin_amdgcn_raw_buffer_load_b32(rs, off, 0, 16); }
DEV void st16(rsrc_t rs, int off, v4u v) { __builtin_amdgcn_raw_buffer_store_b128(v, rs, off, 0, 16); }
DEV void st8(rsrc_t rs, int off, v2u v) { __builtin_amdgcn_raw_buffer_store_b64(v, rs, off, 0, 16); }
DEV void st4(rsrc_t rs, int off, unsigned v) { __builtin_amdgcn_raw_buffer_store_b32(v, rs, off, 0, 16); }
DEV v4u f4_pub(f4 a) {
  return v4u{nosent(__float_as_uint(a[0])), nosent(__float_as_uint(a[1])), nosent(__float_as_uint(a[2])), nosent(__float_as_uint(a[3]))};
}
DEV f4 as_f4(v4u v) { return __builtin_bit_cast(f4, v); }

DEV unsigned max4(v4u v) { const unsigned a = v.x > v.y ? v.x : v.y, b = v.z > v.w ? v.z : v.w; return a > b ? a : b; }
DEV unsigned umax(unsigned a, unsigned b) { return a > b ? a : b; }
template <int S> DEV void ldx4(const unsigned char* ws, unsigned off, v4u& a, v4u& b, v4u& c, v4u& d) {
  constexpr int B = S > 1024 ? -S / 2 : 0;
  asm volatile("global_load_dwordx4 %0, %4, %5 offset:%9 sc1\n\tglobal_load_dwordx4 %1, %4, %5 offset:%6 sc1\n\t"
               "global_load_dwordx4 %2, %4, %5 offset:%7 sc1\n\tglobal_load_dwordx4 %3, %4, %5 offset:%8 sc1"
               : "+v"(a), "+v"(b), "+v"(c), "+v"(d) : "v"(off), "s"(ws), "n"(B + S), "n"(B + 2 * S), "n"(B + 3 * S), "n"(B) : "memory");
}
#define VM0_4(v, o) asm volatile("s_waitcnt vmcnt(0)" : "+v"(v[o]), "+v"(v[o + 1]), "+v"(v[o + 2]), "+v"(v[o + 3]) :: "memory")
#define PIN_4(v, o) asm volatile("" : "+v"(v[o]), "+v"(v[o + 1]), "+v"(v[o + 2]), "+v"(v[o + 3]) :: "memory")
template <int N, int S, bool X> DEV void gather(const unsigned char* ws, unsigned off, v4u (&v)[N], unsigned xoff, unsigned& xv) {
#pragma unroll
  for (int i = 0; i < N; ++i) v[i] = v4u{SENT, SENT, SENT, SENT};
  bool bad = true;
  for (unsigned n = 0; n < SPIN_GLB; ++n) {
    if (bad) {
      if (X) asm volatile("global_load_dword %0, %1, %2 sc1" : "+v"(xv) : "v"(xoff), "s"(ws) : "memory");
#pragma unroll
      for (int i = 0; i < N; i += 4) ldx4<S>(ws, off + i * S, v[i], v[i + 1], v[i + 2], v[i + 3]);
      VM0_4(v, 0);
#pragma unroll
      for (int i = 4; i < N; i += 4) PIN_4(v, i);
      if (X) asm volatile("" : "+v"(xv));
      unsigned mx = X ? xv : 0u;
#pragma unroll
      for (int i = 0; i < N; ++i) mx = umax(mx, max4(v[i]));
      bad = mx == SENT;
    }
    if (!__any(bad)) break;
    __builtin_amdgcn_s_sleep(GSLP);
  }
}

template <int NCB> DEV void gemv_half(lptr L, int tb, int xb, int c0, int lane, f4 (&acc)[NCB]) {
  const int g = lane >> 4, q = (lane >> 2) & 3, p = lane & 3;
  const int rr = 4 * (g & 1) + q;
  const int base = tb + 256 * (4 * g + q) + 8 * p;
#pragma unroll
  for (int ks = 0; ks < 2; ++ks) {
    const v2u x0 = *(LAS v2u*)(L + xb + 64 * ks + 8 * g), x1 = *(LAS v2u*)(L + xb + 64 * ks + 32 + 8 * g);
    const v4u xf = {x0.x, x0.y, x1.x, x1.y};
#pragma unroll
    for (int i = 0; i < NCB; ++i) {
      const int a = base + (((c0 + i) ^ rr) << 5) + 8192 * ks;
      const v2u t0 = __builtin_bit_cast(v2u, __builtin_amdgcn_ds_read_tr16_b64_v4i16((LAS s4*)(L + a)));
      const v2u t1 = __builtin_bit_cast(v2u, __builtin_amdgcn_ds_read_tr16_b64_v4i16((LAS s4*)(L + a + 4096)));
      const v4u wf = {t0.x, t0.y, t1.x, t1.y};
      acc[i] = __builtin_amdgcn_mfma_f32_16x16x32_bf16(__builtin_bit_cast(b8, wf), __builtin_bit_cast(b8, xf), acc[i], 0, 0, 0);
    }
  }
}

template <int STRIDE, bool WAIT> DEV void issue_half(lptr L, int j, const void* base, unsigned vbase, unsigned r4, unsigned lds, int pub) {
  unsigned voff = vbase + r4 * STRIDE + j * 4 * STRIDE;
  unsigned m = (unsigned)(size_t)L + lds + j * 1024;
#pragma unroll
  for (int k = 0; k < 8; ++k) {
    asm volatile("s_mov_b32 m0, %0\n\ts_nop 0\n\tglobal_load_lds_dwordx4 %1, %2" NT_AUX ::"s"(m), "v"(voff), "s"(base) : "memory");
    voff += 8 * STRIDE; m += 2048;
    if (WAIT) waitvm<WIN - 1>();
    if (k == (WIN - 2) % 8 && pub > 0) *flg(L, j) = (unsigned)pub;
  }
}

#define STAMP(i) do { if (i == 0) t_begin = __builtin_amdgcn_s_memrealtime(); else if (i == PI && wave == PW) { *(volatile LAS u64*)(L + L_FLG + 64) = __builtin_amdgcn_s_memrealtime(); } } while (0)
#define STAMP_OUT()

__global__ void __launch_bounds__(512, 2)
fused_decode(const u16* __restrict__ x, const u16* __restrict__ kc, const u16* __restrict__ vc, const u16* __restrict__ wq,
             const u16* __restrict__ wo, const u16* __restrict__ wg, const u16* __restrict__ wu, const u16* __restrict__ wd,
             u16* __restrict__ out, unsigned char* ws) {
  extern __shared__ __attribute__((aligned(16))) unsigned char smem[];
  lptr L = (lptr)smem;
  const int c = blockIdx.x, tid = threadIdx.x, lane = tid & 63, wave = __builtin_amdgcn_readfirstlane(tid >> 6);
  u64 t_begin;
  STAMP(0);
  u64 pc0; asm volatile("s_getpc_b64 %0" : "=s"(pc0));
  const int g = (c & 7) >> 1, m = (c & 1) * 32 + (c >> 3), hh = m >> 3, j8 = m & 7, h = 8 * g + hh;
  const bool has_gu = hh != 0, has_wd = hh != 7;
  const unsigned n_gu = 4, n_wd = has_gu ? 8 : 4, n_wo = n_wd + (has_wd ? 2 : 0), n_all = n_wo + 2;
  const int wo_reg = has_wd ? R2 : R0;
  constexpr int LAG = (WIN - 2) / 8 + 1;

  if (__builtin_expect(wave < 2, 1)) {
    const int j = wave;
    const unsigned r4 = lane >> 4;
    const unsigned sw = ((lane & 15) ^ ((4 * j + r4) << 1)) << 4;
    const char* bq = (const char*)wq + (size_t)j8 * 128 * 8192 + h * 256;
    __builtin_amdgcn_s_setprio(3);
    asm volatile("s_mov_b32 m0, %0\n\ts_nop 0\n\tglobal_load_lds_dwordx4 %1, %2" ::"s"((unsigned)(size_t)L + L_XR + j * 1024), "v"(lane * 16 + j * 1024), "s"(x) : "memory");
    issue_half<8192, false>(L, j, bq, sw, r4, R0, 0);
    issue_half<8192, false>(L, j, bq, sw + 64 * 8192, r4, R0 + 16384, 0);
    BARRIER();
    waitvm<16>(); *flg(L, F_XR0 + j) = 1u;
    const size_t kvo = ((size_t)g * SK + (SK - WKEYS) + m * 64) * 256;
    int i = 2;
    issue_half<256, true>(L, j, (const char*)kc + kvo, sw, r4, R1, i - LAG + 1); ++i;
    STAMP(1);
    issue_half<256, true>(L, j, (const char*)vc + kvo, sw, r4, R1 + 16384, i - LAG + 1); ++i;
    STAMP(2);
    if (has_gu) {
      const int cbg = 7 * g + hh - 1, rbg = j8 & 3;
      const char* bg = (const char*)((j8 & 4) ? wu : wg) + (size_t)rbg * 256 * 7168 + cbg * 256;
      issue_half<7168, true>(L, j, bg, sw, r4, R2, i - LAG + 1); ++i;
      issue_half<7168, true>(L, j, bg, sw + 64 * 7168, r4, R2 + 16384, i - LAG + 1); ++i;
      issue_half<7168, true>(L, j, bg, sw + 128 * 7168, r4, R3, i - LAG + 1); ++i;
      issue_half<7168, true>(L, j, bg, sw + 192 * 7168, r4, R3 + 16384, i - LAG + 1); ++i;
    }
    STAMP(3);
    wait_flag(L, F_WQD, 4);
    if (has_wd) {
      const char* bd = (const char*)wd + (size_t)(7 * g + hh) * 128 * 2048 + j8 * 256;
      issue_half<2048, true>(L, j, bd, sw, r4, R0, i - LAG + 1); ++i;
      issue_half<2048, true>(L, j, bd, sw + 64 * 2048, r4, R0 + 16384, i - LAG + 1); ++i;
      if (has_gu) wait_flag(L, F_GU0, 2);
    }
    STAMP(4);
    const char* bo = (const char*)wo + (size_t)h * 128 * 2048 + j8 * 256;
    issue_half<2048, true>(L, j, bo, sw, r4, wo_reg, i - LAG + 1); ++i;
    issue_half<2048, true>(L, j, bo, sw + 64 * 2048, r4, wo_reg + 16384, i - LAG + 1); ++i;
    STAMP(5);
    if (WIN > 17) { waitvm<16>(); *flg(L, j) = n_all - 2; }
    waitvm<8>(); *flg(L, j) = n_all - 1;
    waitvm<0>(); *flg(L, j) = n_all;
    STAMP(6);
    STAMP_OUT();
    return;
  }

  const rsrc_t rs = __builtin_amdgcn_make_buffer_rsrc(ws, 0, WS_END, 0x00020000);
  if (wave == 7) {
    if (lane < 32) *flg(L, lane) = 0u;
    LGKM0();
  }
  unsigned pf0 = 0;
  if (wave < 2 + CODE_CHUNKS) {
    const unsigned o = (wave - 2) * 8192 + lane * 128;
    pf0 = *(const GAS unsigned*)((pc0 & ~127ull) + (o < CODE_BYTES ? o : 0));
  }

  if (wave >= 6) {
    const int f = wave - 6;
    f4 xres = {0.f, 0.f, 0.f, 0.f};
    if (f == 0) {
      u64 tag;
      asm volatile("s_load_dwordx2 %0, %1, 0x0\n\ts_waitcnt lgkmcnt(0)" : "=s"(tag) : "s"(ws) : "memory");
      tag += 1;
      if (lane == 0) __hip_atomic_store((GAS u64*)(ws + 4000000) + c * 16, t_begin, RLX_AGENT);
      const v4u sv = {SENT, SENT, SENT, SENT};
      const int l5 = lane & 31;
      st16(rs, lane < 32 ? WS_QP + (h * 8 + j8) * 512 + l5 * 16 : WS_FP + ((j8 * 4 + (l5 >> 3)) * 32 + h) * 128 + (l5 & 7) * 16, sv);
      st16(rs, WS_OP + ((8 * g + (lane >> 4)) * 64 + m) * OPS + (lane & 15) * 16, sv);
      st16(rs, WS_OP + ((8 * g + 4 + (lane >> 4)) * 64 + m) * OPS + (lane & 15) * 16, sv);
      if (lane < 8) st4(rs, WS_OP + ((8 * g + lane) * 64 + m) * OPS + 256, SENT);
      if (has_gu && lane < 32) st16(rs, WS_GU + ((7 * g + hh - 1) * 8 + j8) * 512 + lane * 16, sv);
      BARRIER();
      waitvm<0>();
      if (lane == 0) __hip_atomic_store((GAS u64*)(ws + WS_READY) + (g * 64 + m) * 16, tag, RLX_AGENT);
      *flg(L, F_TAGLO) = (unsigned)tag; *flg(L, F_TAGHI) = (unsigned)(tag >> 32);
      set_flag(L, F_CLR, 1);
      STAMP(1);
      wait_land(L, 1);
      {
        bool bad = true;
        for (unsigned n = 0; n < SPIN_GLB; ++n) {
          if (bad) bad = __hip_atomic_load((GAS u64*)(ws + WS_READY) + (g * 64 + lane) * 16, RLX_AGENT) != tag;
          if (!__any(bad)) break;
          __builtin_amdgcn_s_sleep(GSLP);
        }
      }
      set_flag(L, F_GOK, 1);
      STAMP(2);
    } else {
      BARRIER();
      wait_land(L, 1, F_XR0);
      STAMP(2);
      const v4u xa = *(LAS v4u*)(L + L_XR + lane * 32), xb = *(LAS v4u*)(L + L_XR + lane * 32 + 16);
      {
        const v2u xr = *(LAS v2u*)(L + L_XR + (j8 * 128 + g * 32 + (lane & 7) * 4) * 2);
        xres = f4{bf_lo(xr.x), bf_hi(xr.x), bf_lo(xr.y), bf_hi(xr.y)};
      }
      float v[16];
#pragma unroll
      for (int k = 0; k < 4; ++k) { v[2 * k] = bf_lo(xa[k]); v[2 * k + 1] = bf_hi(xa[k]); v[8 + 2 * k] = bf_lo(xb[k]); v[9 + 2 * k] = bf_hi(xb[k]); }
      float s = 0.f;
#pragma unroll
      for (int k = 0; k < 16; ++k) s += v[k];
      const float mean = wave_sum(s) * (1.f / 1024.f);
      float q2 = 0.f;
#pragma unroll
      for (int k = 0; k < 16; ++k) { const float d = v[k] - mean; q2 = fmaf(d, d, q2); }
      const float var = wave_sum(q2) * (1.f / 1024.f);
      const float mb = rbf(mean), rden = 1.0f / rbf(__builtin_sqrtf(rbf(rbf(var) + 1e-5f)));
      v4u o0, o1;
#pragma unroll
      for (int k = 0; k < 4; ++k) {
        o0[k] = pk_bf16(rbf(v[2 * k] - mb) * rden, rbf(v[2 * k + 1] - mb) * rden);
        o1[k] = pk_bf16(rbf(v[8 + 2 * k] - mb) * rden, rbf(v[9 + 2 * k] - mb) * rden);
      }
      *(LAS v4u*)(L + L_XN + lane * 32) = o0;
      *(LAS v4u*)(L + L_XN + lane * 32 + 16) = o1;
      set_flag(L, F_XN, 1);
      STAMP(1);
    }
    if (has_gu) {
      wait_flag(L, F_XN, 1);
      f4 acc[4];
#pragma unroll
      for (int i = 0; i < 4; ++i) acc[i] = f4{0.f, 0.f, 0.f, 0.f};
      const int xb = L_XN + (j8 & 3) * 512;
      wait_land(L, n_gu + 1); gemv_half<4>(L, R2, xb, 4 * f, lane, acc);
      wait_land(L, n_gu + 2); gemv_half<4>(L, R2 + 16384, xb + 128, 4 * f, lane, acc);
      asm volatile("" :: "v"(acc[0]), "v"(acc[1]), "v"(acc[2]), "v"(acc[3]));
      inc_flag(L, F_GU0, lane);
      STAMP(3);
      wait_land(L, n_gu + 3); gemv_half<4>(L, R3, xb + 256, 4 * f, lane, acc);
      wait_land(L, n_gu + 4); gemv_half<4>(L, R3 + 16384, xb + 384, 4 * f, lane, acc);
      const int n = lane & 15;
      const f4 r = n == 0 ? acc[0] : n == 1 ? acc[1] : n == 2 ? acc[2] : acc[3];
      wait_flag(L, F_CLR, 1);
      if (n < 4) st16(rs, WS_GU + ((7 * g + hh - 1) * 8 + j8) * 512 + ((4 * f + n) * 16 + (lane >> 4) * 4) * 4, f4_pub(r));
      STAMP(4);
    }
    if (has_wd) {
      if (f == 0) {
        v4u v[4];
        unsigned dummy = 0; gather<4, 512, false>(ws, WS_GU + ((7 * g + hh) * 8 + (lane >> 5) * 4) * 512 + (lane & 31) * 16, v, 0, dummy);
        STAMP(5);
        const f4 s = (as_f4(v[0]) + as_f4(v[1])) + (as_f4(v[2]) + as_f4(v[3]));
        float hv[4];
#pragma unroll
        for (int k = 0; k < 4; ++k) {
          auto sw = __builtin_amdgcn_permlane32_swap(__float_as_uint(s[k]), __float_as_uint(s[k]), false, false);
          const float gt = rbf(__uint_as_float(sw[0])), up = rbf(__uint_as_float(sw[1]));
          const float sg = rbf(gt * __builtin_amdgcn_rcpf(1.f + __expf(-gt)));
          hv[k] = sg * up;
        }
        if (lane < 32) *(LAS v2u*)(L + L_HV + lane * 8) = v2u{pk_bf16(hv[0], hv[1]), pk_bf16(hv[2], hv[3])};
        set_flag(L, F_HV, 1);
      } else {
        wait_flag(L, F_HV, 1);
      }
      f4 acc[4];
#pragma unroll
      for (int i = 0; i < 4; ++i) acc[i] = f4{0.f, 0.f, 0.f, 0.f};
      wait_land(L, n_wd + 1); gemv_half<4>(L, R0, L_HV, 4 * f, lane, acc);
      wait_land(L, n_wd + 2); gemv_half<4>(L, R0 + 16384, L_HV + 128, 4 * f, lane, acc);
      const int n = lane & 15;
      const f4 r = n == 0 ? acc[0] : n == 1 ? acc[1] : n == 2 ? acc[2] : acc[3];
      if (n < 4) *(LAS f4*)(L + L_FP + ((4 * f + n) * 16 + (lane >> 4) * 4) * 4) = r;
      inc_flag(L, F_FFN, lane);
      STAMP(6);
    }
    if (hh == 0 && f == 1) {
      wait_flag(L, F_CLR, 1);
      const u64 tag = (u64)*flg(L, F_TAGLO) | ((u64)*flg(L, F_TAGHI) << 32);
      for (unsigned n = 0; n < SPIN_GLB; ++n) {
        bool ok = true;
#pragma unroll
        for (int i = 0; i < 4; ++i) ok &= __hip_atomic_load((GAS u64*)(ws + WS_READY) + (i * 64 + lane) * 16, RLX_AGENT) == tag;
        if (__all(ok)) break;
        __builtin_amdgcn_s_sleep(GSLP);
      }
      CFENCE();
      if (c == 0 && lane == 0) __hip_atomic_store((GAS u64*)(ws + WS_EPOCH), tag, RLX_AGENT);
      STAMP(7);
      v4u v[4];
      unsigned dummy = 0; gather<4, 1024, false>(ws, WS_FP + (j8 * 4 + g) * 4096 + lane * 16, v, 0, dummy);
      STAMP(8);
      f4 s = (as_f4(v[0]) + as_f4(v[1])) + (as_f4(v[2]) + as_f4(v[3]));
#pragma unroll
      for (int k = 0; k < 4; ++k) { float t = s[k]; t += dpp<ROR8>(t); s[k] = x32_sum(x16_sum(t)) + xres[k]; }
      if (lane < 8) *(GAS v2u*)(out + j8 * 128 + g * 32 + lane * 4) = v2u{pk_bf16(s[0], s[1]), pk_bf16(s[2], s[3])};
      STAMP(9);
      if (c == PC) {
        LGKM0();
        const u64 tp = *(volatile LAS u64*)(L + L_FLG + 64);
        const u64 now = __builtin_amdgcn_s_memrealtime();
        int mn = 1 << 30, mx = -(1 << 30);
        for (int i = 0; i < 4; ++i) { const int t = (int)(__hip_atomic_load((GAS u64*)(ws + 4000000) + (i * 64 + lane) * 16, RLX_AGENT) - t_begin); mn = t < mn ? t : mn; mx = t > mx ? t : mx; }
        for (int o = 32; o > 0; o >>= 1) { const int a = __shfl_xor(mn, o), b = __shfl_xor(mx, o); mn = a < mn ? a : mn; mx = b > mx ? b : mx; }
        u64 d = 500 + (u64)__builtin_amdgcn_readfirstlane(mx - mn) * 0; if (d > 20000) d = 20000;
        while (__builtin_amdgcn_s_memrealtime() < now + d) __builtin_amdgcn_s_sleep(1);
      }
    }
    STAMP_OUT();
    return;
  }

  const int w = wave - 2, n16 = lane & 15, g4 = lane >> 4;
  BARRIER();
  asm volatile("" :: "v"(pf0));
  __builtin_amdgcn_s_setprio(CPRIO);
  {
    f4 acc[2] = {f4{0.f, 0.f, 0.f, 0.f}, f4{0.f, 0.f, 0.f, 0.f}};
    wait_flag(L, F_XN, 1);
    wait_land(L, 1); gemv_half<2>(L, R0, L_XN + j8 * 256, 2 * w, lane, acc);
    wait_flag(L, F_CLR, 1);
    wait_land(L, 2);
    STAMP(1);
    gemv_half<2>(L, R0 + 16384, L_XN + j8 * 256 + 128, 2 * w, lane, acc);
    const f4 r = n16 == 0 ? acc[0] : acc[1];
    if (n16 < 2) st16(rs, WS_QP + (h * 8 + j8) * 512 + ((2 * w + n16) * 16 + g4 * 4) * 4, f4_pub(r));
    inc_flag(L, F_WQD, lane);
    STAMP(2);
  }
  {
    wait_flag(L, F_GOK, 1);
    v4u v[8];
    unsigned dummy = 0; gather<8, 1024, false>(ws, WS_QP + (8 * g + 2 * w) * 4096 + lane * 16, v, 0, dummy);
    STAMP(3);
    const f4 a = (as_f4(v[0]) + as_f4(v[1])) + (as_f4(v[2]) + as_f4(v[3]));
    const f4 b = (as_f4(v[4]) + as_f4(v[5])) + (as_f4(v[6]) + as_f4(v[7]));
    float qv[4];
#pragma unroll
    for (int k = 0; k < 4; ++k) {
      auto sw = __builtin_amdgcn_permlane32_swap(__float_as_uint(a[k]), __float_as_uint(b[k]), false, false);
      qv[k] = __uint_as_float(sw[0]) + __uint_as_float(sw[1]);
    }
    *(LAS v2u*)(L + L_Q + (2 * w + (lane >> 5)) * 288 + (lane & 31) * 8) = v2u{pk_bf16(qv[0], qv[1]), pk_bf16(qv[2], qv[3])};
    inc_flag(L, F_QC, lane);
  }
  {
    wait_land(L, 3);
    v4u kf[4];
    const int krow = 16 * w + n16;
#pragma unroll
    for (int ks = 0; ks < 4; ++ks) kf[ks] = *(LAS v4u*)(L + R1 + krow * 256 + (((4 * ks + g4) ^ ((n16 & 7) << 1)) << 4));
    wait_flag(L, F_QC, 4);
    STAMP(4);
    f4 s = {0.f, 0.f, 0.f, 0.f};
#pragma unroll
    for (int ks = 0; ks < 4; ++ks) {
      const v4u qf = *(LAS v4u*)(L + L_Q + (n16 & 7) * 288 + ks * 64 + g4 * 16);
      s = __builtin_amdgcn_mfma_f32_16x16x32_bf16(__builtin_bit_cast(b8, kf[ks]), __builtin_bit_cast(b8, qf), s, 0, 0, 0);
    }
    float sc[4];
#pragma unroll
    for (int r = 0; r < 4; ++r) sc[r] = rbf(rbf(s[r]) * 0.08838834764831845f);
    const float mw = x32_max(x16_max(fmaxf(fmaxf(sc[0], sc[1]), fmaxf(sc[2], sc[3]))));
    float p[4];
#pragma unroll
    for (int r = 0; r < 4; ++r) p[r] = __builtin_amdgcn_exp2f((sc[r] - mw) * 1.4426950408889634f);
    const float lw = x32_sum(x16_sum((p[0] + p[1]) + (p[2] + p[3])));
    const v2u pf = {pk_bf16(p[0], p[1]), pk_bf16(p[2], p[3])};
    wait_land(L, 4);
    const int q = (lane >> 2) & 3, pp = lane & 3, rr = 4 * (g4 & 1) + q;
    const int vb = R1 + 16384 + (16 * w + 4 * g4 + q) * 256 + 8 * pp;
    if (lane < 8) *(LAS v2u*)(L + L_ML + (lane * 4 + w) * 8) = v2u{__float_as_uint(mw), __float_as_uint(lw)};
#pragma unroll
    for (int db = 0; db < 8; ++db) {
      const s4 vf = __builtin_amdgcn_ds_read_tr16_b64_v4i16((LAS s4*)(L + vb + ((db ^ rr) << 5)));
      const f4 o = __builtin_amdgcn_mfma_f32_16x16x16bf16_1k(vf, __builtin_bit_cast(s4, pf), f4{0.f, 0.f, 0.f, 0.f}, 0, 0, 0);
      if (n16 < 8) *(LAS f4*)(L + L_OW + (w * 8 + n16) * 528 + (db * 16 + g4 * 4) * 4) = o;
    }
    inc_flag(L, F_OC, lane);
    wait_flag(L, F_OC, 4);
    STAMP(5);
    const int hd = 2 * w + (lane >> 5);
    const v4u ml0 = *(LAS v4u*)(L + L_ML + hd * 32), ml1 = *(LAS v4u*)(L + L_ML + hd * 32 + 16);
    const float m0 = __uint_as_float(ml0.x), m1 = __uint_as_float(ml0.z), m2 = __uint_as_float(ml1.x), m3 = __uint_as_float(ml1.z);
    const float M = fmaxf(fmaxf(m0, m1), fmaxf(m2, m3));
    const float e0 = __builtin_amdgcn_exp2f((m0 - M) * 1.4426950408889634f), e1 = __builtin_amdgcn_exp2f((m1 - M) * 1.4426950408889634f),
                e2 = __builtin_amdgcn_exp2f((m2 - M) * 1.4426950408889634f), e3 = __builtin_amdgcn_exp2f((m3 - M) * 1.4426950408889634f);
    const float Ls = (e0 * __uint_as_float(ml0.y) + e1 * __uint_as_float(ml0.w)) + (e2 * __uint_as_float(ml1.y) + e3 * __uint_as_float(ml1.w));
    const float inv = __builtin_amdgcn_rcpf(Ls);
    const int ob = L_OW + hd * 528 + (lane & 31) * 16;
    const f4 o = (*(LAS f4*)(L + ob) * e0 + *(LAS f4*)(L + ob + 8 * 528) * e1) + (*(LAS f4*)(L + ob + 16 * 528) * e2 + *(LAS f4*)(L + ob + 24 * 528) * e3);
    st8(rs, WS_OP + ((8 * g + hd) * 64 + m) * OPS + (lane & 31) * 8,
        v2u{nosent(pk_bf16(o[0] * inv, o[1] * inv)), nosent(pk_bf16(o[2] * inv, o[3] * inv))});
    if ((lane & 31) == 0) st4(rs, WS_OP + ((8 * g + hd) * 64 + m) * OPS + 256, nosent(__float_as_uint(fmaf(M, 1.4426950408889634f, __builtin_amdgcn_logf(Ls)))));
    STAMP(6);
  }
  if (w == 0) {
    unsigned lse = SENT;
    v4u v[16];
    gather<16, 4 * OPS, true>(ws, WS_OP + (h * 64 + g4) * OPS + n16 * 16 + 2 * OPS, v, WS_OP + (h * 64 + lane) * OPS + 256, lse);
    STAMP(7);
    const float lf = __uint_as_float(lse);
    const float M = wave_max(lf);
    const float e = __builtin_amdgcn_exp2f(lf - M);
    const float wt = e * __builtin_amdgcn_rcpf(wave_sum(e));
    const float hi = rbf(wt);
    *(LAS unsigned*)(L + L_WT + ((lane & 3) * 16 + (lane >> 2)) * 4) = pk_bf16(wt, wt);
    *(LAS unsigned*)(L + L_WT + 256 + ((lane & 3) * 16 + (lane >> 2)) * 4) = pk_bf16(wt - hi, wt - hi);
    LGKM0();
    const unsigned hm = (lane & 1) ? 0xffff0000u : 0x0000ffffu;
    const int dsel = (lane & 7) >> 1;
    const unsigned k0 = dsel == 0 ? hm : 0u, k1 = dsel == 1 ? hm : 0u, k2 = dsel == 2 ? hm : 0u, k3 = dsel == 3 ? hm : 0u;
    f4 acc = {0.f, 0.f, 0.f, 0.f};
#pragma unroll
    for (int i4 = 0; i4 < 4; ++i4) {
      const v4u t = *(LAS v4u*)(L + L_WT + ((lane >> 3) & 1) * 256 + g4 * 64 + i4 * 16);
#pragma unroll
      for (int k = 0; k < 4; ++k) {
        const v4u af = {t[k] & k0, t[k] & k1, t[k] & k2, t[k] & k3};
        acc = __builtin_amdgcn_mfma_f32_16x16x32_bf16(__builtin_bit_cast(b8, af), __builtin_bit_cast(b8, v[i4 * 4 + k]), acc, 0, 0, 0);
      }
    }
    float o[4];
#pragma unroll
    for (int k = 0; k < 4; ++k) o[k] = x32_sum(acc[k]);
    if (lane < 32) *(LAS v2u*)(L + L_OV + (8 * n16 + 4 * g4) * 2) = v2u{pk_bf16(o[0], o[1]), pk_bf16(o[2], o[3])};
    set_flag(L, F_OV, 1);
  } else {
    wait_flag(L, F_OV, 1);
  }
  STAMP(8);
  {
    f4 acc[2] = {f4{0.f, 0.f, 0.f, 0.f}, f4{0.f, 0.f, 0.f, 0.f}};
    wait_land(L, n_wo + 1); gemv_half<2>(L, wo_reg, L_OV, 2 * w, lane, acc);
    f4 fp = {0.f, 0.f, 0.f, 0.f};
    if (has_wd) {
      wait_flag(L, F_FFN, 2);
      fp = *(LAS f4*)(L + L_FP + ((2 * w + (n16 & 1)) * 16 + g4 * 4) * 4);
    }
    wait_land(L, n_wo + 2);
    STAMP(9);
    gemv_half<2>(L, wo_reg + 16384, L_OV + 128, 2 * w, lane, acc);
    const f4 r = (n16 == 0 ? acc[0] : acc[1]) + fp;
    if (n16 < 2) st16(rs, WS_FP + ((j8 * 4 + w) * 32 + h) * 128 + (n16 * 16 + g4 * 4) * 4, f4_pub(r));
    STAMP(10);
  }
  STAMP_OUT();
}

extern "C" void kernel_launch(void* const* d_in, const int* in_sizes, int n_in, void* d_out, int out_size, void* d_ws,
                              size_t ws_size, hipStream_t stream) {
  (void)in_sizes; (void)n_in; (void)out_size; (void)ws_size;
  (void)hipFuncSetAttribute((const void*)fused_decode, hipFuncAttributeMaxDynamicSharedMemorySize, LDS_BYTES);
  hipLaunchKernelGGL(fused_decode, dim3(256), dim3(512), LDS_BYTES, stream, (const u16*)d_in[0], (const u16*)d_in[1],
                     (const u16*)d_in[2], (const u16*)d_in[3], (const u16*)d_in[4], (const u16*)d_in[6], (const u16*)d_in[7],
                     (const u16*)d_in[8], (u16*)d_out, (unsigned char*)d_ws);
}
```

```cpp
#include <hip/hip_runtime.h>
#include <stdint.h>

constexpr int WIN_KV = 18;
constexpr int WIN_G1 = 10;
constexpr int WIN_G2 = 12;
constexpr int WIN_WO = 12;
constexpr int WIN_WD = 12;
constexpr int NG1 = 6;
constexpr int QD = 12;
constexpr int IDLY = 2;
constexpr int T_T = 130;
constexpr int T_O = 642;
constexpr int T_H = 672;
constexpr int T_F = 818;
#define SLP 1
#define NT_AUX " nt"

typedef unsigned short u16;
typedef unsigned long long u64;
typedef unsigned v16u __attribute__((ext_vector_type(16)));
typedef unsigned v8u __attribute__((ext_vector_type(8)));
typedef unsigned v4u __attribute__((ext_vector_type(4)));
typedef unsigned v2u __attribute__((ext_vector_type(2)));
typedef float f4 __attribute__((ext_vector_type(4)));
typedef float f2 __attribute__((ext_vector_type(2)));
typedef short s4 __attribute__((ext_vector_type(4)));
typedef __bf16 b8 __attribute__((ext_vector_type(8)));
typedef __bf16 b2 __attribute__((ext_vector_type(2)));
#define GAS __attribute__((address_space(1)))
#define LAS __attribute__((address_space(3)))
#define RLX_AGENT __ATOMIC_RELAXED, __HIP_MEMORY_SCOPE_AGENT
#define DEV __device__ __forceinline__

constexpr int SK = 32768, WKEYS = 4096;
constexpr int WS_EPOCH = 0;
constexpr int OPS = 640;
constexpr int WS_QP = 4096;
constexpr int WS_GU = WS_QP + 32 * 6 * 512;
constexpr int WS_OP = WS_GU + 4 * 42 * 512;
constexpr int WS_FP = WS_OP + 32 * 16 * OPS;
constexpr int T0 = 0, T1 = 32768, T2 = 65536, TG = 49152;
constexpr int L_XN = 139264;
constexpr int L_XR = L_XN + 2048;
constexpr int L_Q = L_XR + 2048;
constexpr int L_ML = L_Q + 8 * 288;
constexpr int L_P = L_ML + 512;
constexpr int L_OV = L_P + 4096;
constexpr int L_HV = L_OV + 256;
constexpr int L_FLG = L_HV + 512;
constexpr int L_DUM = L_FLG + 128;
constexpr int LDS_BYTES = L_DUM + 256;
static_assert(LDS_BYTES <= 160 * 1024, "lds");
enum { F_LAND0 = 0, F_LAND1 = 1, F_XR0 = 2, F_XR1 = 3, F_EP = 4, F_XN = 6, F_QC = 7, F_OC = 8, F_OV = 9, F_WQD = 10, F_GU4 = 11, F_LC = 12, F_HV = 13 };

constexpr unsigned CODE_BYTES = 13312;
constexpr unsigned SPIN_LDS = 1u << 25;
constexpr float LOG2E = 1.4426950408889634f;

DEV float bf_lo(unsigned v) { return __uint_as_float(v << 16); }
DEV float bf_hi(unsigned v) { return __uint_as_float(v & 0xffff0000u); }
DEV unsigned pk_bf16(float lo, float hi) {
  b2 r; r[0] = (__bf16)lo; r[1] = (__bf16)hi; return __builtin_bit_cast(unsigned, r);
}
DEV unsigned pko(float lo, float hi) { unsigned r = pk_bf16(lo, hi); asm("" : "+v"(r)); return r; }
DEV float fmx(float a, float b) { return __builtin_elementwise_maximum(a, b); }
DEV float rbf(float x) { return __uint_as_float(pk_bf16(x, x) << 16); }
template <int CTRL> DEV float dpp(float x) {
  return __builtin_bit_cast(float, __builtin_amdgcn_mov_dpp(__builtin_bit_cast(int, x), CTRL, 0xf, 0xf, true));
}
constexpr int XOR1 = 0xB1, XOR2 = 0x4E, HMIR = 0x141, ROR8 = 0x128;
DEV float x16_sum(float x) {
  auto s = __builtin_amdgcn_permlane16_swap(__float_as_uint(x), __float_as_uint(x), false, false);
  return __uint_as_float(s[0]) + __uint_as_float(s[1]);
}
DEV float x32_sum(float x) {
  auto s = __builtin_amdgcn_permlane32_swap(__float_as_uint(x), __float_as_uint(x), false, false);
  return __uint_as_float(s[0]) + __uint_as_float(s[1]);
}
DEV float x16_max(float x) {
  auto s = __builtin_amdgcn_permlane16_swap(__float_as_uint(x), __float_as_uint(x), false, false);
  return fmx(__uint_as_float(s[0]), __uint_as_float(s[1]));
}
DEV float x32_max(float x) {
  auto s = __builtin_amdgcn_permlane32_swap(__float_as_uint(x), __float_as_uint(x), false, false);
  return fmx(__uint_as_float(s[0]), __uint_as_float(s[1]));
}
DEV float row_sum(float x) { x += dpp<XOR1>(x); x += dpp<XOR2>(x); x += dpp<HMIR>(x); x += dpp<ROR8>(x); return x; }
DEV float row_max(float x) { x = fmx(x, dpp<XOR1>(x)); x = fmx(x, dpp<XOR2>(x)); x = fmx(x, dpp<HMIR>(x)); return fmx(x, dpp<ROR8>(x)); }
DEV float wave_sum(float x) { return x32_sum(x16_sum(row_sum(x))); }
#define SCHED0() __builtin_amdgcn_sched_barrier(0)
#define CFENCE() asm volatile("" ::: "memory")
#define PIN(x) asm volatile("" : "+v"(x))
#define PIN_4(a, b, c, d) asm volatile("" : "+v"(a), "+v"(b), "+v"(c), "+v"(d) :: "memory")
template <int N> DEV void waitvm() { asm volatile("s_waitcnt vmcnt(%0)" ::"n"(N) : "memory"); }

DEV unsigned since(unsigned t0) { return ((unsigned)__builtin_amdgcn_s_memrealtime() - t0) & 0x3fffffffu; }
DEV void wait_until(unsigned t0, unsigned t) {
#pragma nounroll
  for (;;) { const unsigned n = since(t0); if (n >= t) break; if (n + 30 < t) __builtin_amdgcn_s_sleep(4); }
}
typedef LAS unsigned char* lptr;
DEV volatile LAS unsigned* flg(lptr L, int i) { return (volatile LAS unsigned*)(L + L_FLG) + i; }
template <int SL, int OFF> DEV void wait_lds(unsigned la, int thr) {
  unsigned t, cnt;
  asm volatile("s_mov_b32 %1, %4\n"
               "1:\n\tds_read_b32 %0, %2 offset:%6\n\ts_waitcnt lgkmcnt(0)\n\tv_cmp_gt_i32 vcc, %3, %0\n\ts_cbranch_vccz 2f\n\t"
               "s_sleep %5\n\ts_sub_u32 %1, %1, 1\n\ts_cbranch_scc0 1b\n2:"
               : "=&v"(t), "=&s"(cnt) : "v"(la), "s"(thr), "n"(SPIN_LDS), "n"(SL), "n"(OFF) : "vcc", "scc", "memory");
}
DEV void wait_vec(unsigned la, int thr) {
  unsigned t, cnt;
  asm volatile("s_mov_b32 %1, %4\n"
               "1:\n\tds_read_b32 %0, %2\n\ts_waitcnt lgkmcnt(0)\n\tv_cmp_gt_i32 vcc, %3, %0\n\ts_cbranch_vccz 2f\n\t"
               "s_sub_u32 %1, %1, 1\n\ts_cbranch_scc0 1b\n2:"
               : "=&v"(t), "=&s"(cnt) : "v"(la), "v"(thr), "n"(SPIN_LDS) : "vcc", "scc", "memory");
}
#define wait_flag(i, v) wait_lds<SLP, (i) * 4>(fl1, v)
#define wait_flag0(i, v) wait_lds<0, (i) * 4>(fl1, v)
#define wait_land(v) wait_lds<SLP, 0>(fl2, v)
#define wait_land0(v) wait_lds<0, 0>(fl2, v)
DEV void set_flag(lptr L, int i, unsigned v) { CFENCE(); *flg(L, i) = v; }
DEV void inc_flag(lptr L, int i) {
  asm volatile("s_mov_b64 exec, 1\n\tds_add_u32 %0, %1\n\ts_mov_b64 exec, -1" ::"v"((unsigned)(size_t)L + L_FLG + i * 4), "v"(1u) : "memory");
}
DEV void st16m(unsigned char* ws, unsigned off, v4u v, u64 m) {
  asm volatile("s_mov_b64 exec, %3\n\tglobal_store_dwordx4 %0, %1, %2 sc1\n\ts_mov_b64 exec, -1\n\ts_nop 0" ::"v"(off), "v"(v), "s"(ws), "s"(m) : "memory");
}
DEV v4u tagbytes(unsigned tag) { v4u t = {tag & 255u, (tag >> 8) & 255u, (tag >> 16) & 255u, tag >> 24}; asm volatile("" : "+v"(t)); return t; }
DEV v2u tagpair(unsigned tag, int lane) { const unsigned t = tag >> (16 * (lane & 1)); v2u r = {t & 255u, (t >> 8) & 255u}; asm volatile("" : "+v"(r)); return r; }
DEV v4u enc4(f4 a, v4u tb) { return (__builtin_bit_cast(v4u, a) & ~255u) | tb; }
template <int I, typename V> DEV f4 quad(const V& v) { return f4{__uint_as_float(v[4 * I]), __uint_as_float(v[4 * I + 1]), __uint_as_float(v[4 * I + 2]), __uint_as_float(v[4 * I + 3])}; }

DEV void issue_q(const unsigned char* ws, unsigned o0, v16u& d0, v8u& d1) {
  asm volatile(
      "global_load_dwordx4 v[128:131], %3, %2 offset:-3072 sc1\n\t"
      "global_load_dwordx4 v[132:135], %3, %2 offset:-2048 sc1\n\t"
      "global_load_dwordx4 v[136:139], %3, %2 offset:-1024 sc1\n\t"
      "global_load_dwordx4 v[140:143], %3, %2 offset:0 sc1\n\t"
      "global_load_dwordx4 v[144:147], %3, %2 offset:1024 sc1\n\t"
      "global_load_dwordx4 v[148:151], %3, %2 offset:2048 sc1\n\t"
      : "=&{v[128:143]}"(d0), "=&{v[144:151]}"(d1)
      : "s"(ws), "v"(o0) : "memory");
}
DEV unsigned finish_q(const unsigned char* ws, unsigned o0, unsigned tag, v16u& d0, v8u& d1) {
  unsigned t, cnt;
  asm volatile(
      "s_mov_b32 %3, 0\n\t"
      "1:\n\t"
      "s_waitcnt vmcnt(5)\n\t"
      "v_xor_b32 v128, %6, v128\n\t"
      "v_xor_b32 v129, %7, v129\n\t"
      "v_xor_b32 v130, %8, v130\n\t"
      "v_xor_b32 v131, %9, v131\n\t"
      "v_or_b32 %2, v128, v129\n\t"
      "v_or3_b32 %2, %2, v130, v131\n\t"
      "s_waitcnt vmcnt(4)\n\t"
      "v_xor_b32 v132, %6, v132\n\t"
      "v_xor_b32 v133, %7, v133\n\t"
      "v_xor_b32 v134, %8, v134\n\t"
      "v_xor_b32 v135, %9, v135\n\t"
      "v_or3_b32 %2, %2, v132, v133\n\t"
      "v_or3_b32 %2, %2, v134, v135\n\t"
      "s_waitcnt vmcnt(3)\n\t"
      "v_xor_b32 v136, %6, v136\n\t"
      "v_xor_b32 v137, %7, v137\n\t"
      "v_xor_b32 v138, %8, v138\n\t"
      "v_xor_b32 v139, %9, v139\n\t"
      "v_or3_b32 %2, %2, v136, v137\n\t"
      "v_or3_b32 %2, %2, v138, v139\n\t"
      "s_waitcnt vmcnt(2)\n\t"
      "v_xor_b32 v140, %6, v140\n\t"
      "v_xor_b32 v141, %7, v141\n\t"
      "v_xor_b32 v142, %8, v142\n\t"
      "v_xor_b32 v143, %9, v143\n\t"
      "v_or3_b32 %2, %2, v140, v141\n\t"
      "v_or3_b32 %2, %2, v142, v143\n\t"
      "s_waitcnt vmcnt(1)\n\t"
      "v_xor_b32 v144, %6, v144\n\t"
      "v_xor_b32 v145, %7, v145\n\t"
      "v_xor_b32 v146, %8, v146\n\t"
      "v_xor_b32 v147, %9, v147\n\t"
      "v_or3_b32 %2, %2, v144, v145\n\t"
      "v_or3_b32 %2, %2, v146, v147\n\t"
      "s_waitcnt vmcnt(0)\n\t"
      "v_xor_b32 v148, %6, v148\n\t"
      "v_xor_b32 v149, %7, v149\n\t"
      "v_xor_b32 v150, %8, v150\n\t"
      "v_xor_b32 v151, %9, v151\n\t"
      "v_or3_b32 %2, %2, v148, v149\n\t"
      "v_or3_b32 %2, %2, v150, v151\n\t"
      "v_and_b32 %2, 0xff, %2\n\t"
      "v_cmp_ne_u32 vcc, 0, %2\n\t"
      "s_cbranch_vccz 2f\n\t"
      "s_add_u32 %3, %3, 1\n\t"
      "s_bitcmp1_b32 %3, 20\n\t"
      "s_cbranch_scc1 2f\n\t"
      "s_sleep 1\n\t"
      "v_or3_b32 %2, v128, v129, v130\n\t"
      "v_or_b32 %2, %2, v131\n\t"
      "v_and_b32 %2, 0xff, %2\n\t"
      "v_cmp_ne_u32 vcc, 0, %2\n\t"
      "v_xor_b32 v128, %6, v128\n\t"
      "v_xor_b32 v129, %7, v129\n\t"
      "v_xor_b32 v130, %8, v130\n\t"
      "v_xor_b32 v131, %9, v131\n\t"
      "s_wqm_b64 exec, vcc\n\t"
      "global_load_dwordx4 v[128:131], %4, %5 offset:-3072 sc1\n\t"
      "s_mov_b64 exec, -1\n\t"
      "v_or3_b32 %2, v132, v133, v134\n\t"
      "v_or_b32 %2, %2, v135\n\t"
      "v_and_b32 %2, 0xff, %2\n\t"
      "v_cmp_ne_u32 vcc, 0, %2\n\t"
      "v_xor_b32 v132, %6, v132\n\t"
      "v_xor_b32 v133, %7, v133\n\t"
      "v_xor_b32 v134, %8, v134\n\t"
      "v_xor_b32 v135, %9, v135\n\t"
      "s_wqm_b64 exec, vcc\n\t"
      "global_load_dwordx4 v[132:135], %4, %5 offset:-2048 sc1\n\t"
      "s_mov_b64 exec, -1\n\t"
      "v_or3_b32 %2, v136, v137, v138\n\t"
      "v_or_b32 %2, %2, v139\n\t"
      "v_and_b32 %2, 0xff, %2\n\t"
      "v_cmp_ne_u32 vcc, 0, %2\n\t"
      "v_xor_b32 v136, %6, v136\n\t"
      "v_xor_b32 v137, %7, v137\n\t"
      "v_xor_b32 v138, %8, v138\n\t"
      "v_xor_b32 v139, %9, v139\n\t"
      "s_wqm_b64 exec, vcc\n\t"
      "global_load_dwordx4 v[136:139], %4, %5 offset:-1024 sc1\n\t"
      "s_mov_b64 exec, -1\n\t"
      "v_or3_b32 %2, v140, v141, v142\n\t"
      "v_or_b32 %2, %2, v143\n\t"
      "v_and_b32 %2, 0xff, %2\n\t"
      "v_cmp_ne_u32 vcc, 0, %2\n\t"
      "v_xor_b32 v140, %6, v140\n\t"
      "v_xor_b32 v141, %7, v141\n\t"
      "v_xor_b32 v142, %8, v142\n\t"
      "v_xor_b32 v143, %9, v143\n\t"
      "s_wqm_b64 exec, vcc\n\t"
      "global_load_dwordx4 v[140:143], %4, %5 offset:0 sc1\n\t"
      "s_mov_b64 exec, -1\n\t"
      "v_or3_b32 %2, v144, v145, v146\n\t"
      "v_or_b32 %2, %2, v147\n\t"
      "v_and_b32 %2, 0xff, %2\n\t"
      "v_cmp_ne_u32 vcc, 0, %2\n\t"
      "v_xor_b32 v144, %6, v144\n\t"
      "v_xor_b32 v145, %7, v145\n\t"
      "v_xor_b32 v146, %8, v146\n\t"
      "v_xor_b32 v147, %9, v147\n\t"
      "s_wqm_b64 exec, vcc\n\t"
      "global_load_dwordx4 v[144:147], %4, %5 offset:1024 sc1\n\t"
      "s_mov_b64 exec, -1\n\t"
      "v_or3_b32 %2, v148, v149, v150\n\t"
      "v_or_b32 %2, %2, v151\n\t"
      "v_and_b32 %2, 0xff, %2\n\t"
      "v_cmp_ne_u32 vcc, 0, %2\n\t"
      "v_xor_b32 v148, %6, v148\n\t"
      "v_xor_b32 v149, %7, v149\n\t"
      "v_xor_b32 v150, %8, v150\n\t"
      "v_xor_b32 v151, %9, v151\n\t"
      "s_wqm_b64 exec, vcc\n\t"
      "global_load_dwordx4 v[148:151], %4, %5 offset:2048 sc1\n\t"
      "s_mov_b64 exec, -1\n\t"
      "s_waitcnt vmcnt(0)\n\t"
      "s_branch 1b\n\t"
      "2:\n\t"
      : "+{v[128:143]}"(d0), "+{v[144:151]}"(d1), "=&v"(t), "=&s"(cnt), "+v"(o0)
      : "s"(ws), "s"(tag & 255u), "s"((tag >> 8) & 255u), "s"((tag >> 16) & 255u), "s"(tag >> 24) : "vcc", "scc", "memory");
  return cnt;
}
DEV void issue_h(const unsigned char* ws, unsigned o0, v8u& d0, v4u& d1) {
  asm volatile(
      "global_load_dwordx4 v[128:131], %3, %2 offset:0 sc1\n\t"
      "global_load_dwordx4 v[132:135], %3, %2 offset:512 sc1\n\t"
      "global_load_dwordx4 v[136:139], %3, %2 offset:1024 sc1\n\t"
      : "=&{v[128:135]}"(d0), "=&{v[136:139]}"(d1)
      : "s"(ws), "v"(o0) : "memory");
}
DEV unsigned finish_h(const unsigned char* ws, unsigned o0, unsigned tag, v8u& d0, v4u& d1) {
  unsigned t, cnt;
  asm volatile(
      "s_mov_b32 %3, 0\n\t"
      "1:\n\t"
      "s_waitcnt vmcnt(2)\n\t"
      "v_xor_b32 v128, %6, v128\n\t"
      "v_xor_b32 v129, %7, v129\n\t"
      "v_xor_b32 v130, %8, v130\n\t"
      "v_xor_b32 v131, %9, v131\n\t"
      "v_or_b32 %2, v128, v129\n\t"
      "v_or3_b32 %2, %2, v130, v131\n\t"
      "s_waitcnt vmcnt(1)\n\t"
      "v_xor_b32 v132, %6, v132\n\t"
      "v_xor_b32 v133, %7, v133\n\t"
      "v_xor_b32 v134, %8, v134\n\t"
      "v_xor_b32 v135, %9, v135\n\t"
      "v_or3_b32 %2, %2, v132, v133\n\t"
      "v_or3_b32 %2, %2, v134, v135\n\t"
      "s_waitcnt vmcnt(0)\n\t"
      "v_xor_b32 v136, %6, v136\n\t"
      "v_xor_b32 v137, %7, v137\n\t"
      "v_xor_b32 v138, %8, v138\n\t"
      "v_xor_b32 v139, %9, v139\n\t"
      "v_or3_b32 %2, %2, v136, v137\n\t"
      "v_or3_b32 %2, %2, v138, v139\n\t"
      "v_and_b32 %2, 0xff, %2\n\t"
      "v_cmp_ne_u32 vcc, 0, %2\n\t"
      "s_cbranch_vccz 2f\n\t"
      "s_add_u32 %3, %3, 1\n\t"
      "s_bitcmp1_b32 %3, 20\n\t"
      "s_cbranch_scc1 2f\n\t"
      "s_sleep 2\n\t"
      "v_or3_b32 %2, v128, v129, v130\n\t"
      "v_or_b32 %2, %2, v131\n\t"
      "v_and_b32 %2, 0xff, %2\n\t"
      "v_cmp_ne_u32 vcc, 0, %2\n\t"
      "v_xor_b32 v128, %6, v128\n\t"
      "v_xor_b32 v129, %7, v129\n\t"
      "v_xor_b32 v130, %8, v130\n\t"
      "v_xor_b32 v131, %9, v131\n\t"
      "s_wqm_b64 exec, vcc\n\t"
      "global_load_dwordx4 v[128:131], %4, %5 offset:0 sc1\n\t"
      "s_mov_b64 exec, -1\n\t"
      "v_or3_b32 %2, v132, v133, v134\n\t"
      "v_or_b32 %2, %2, v135\n\t"
      "v_and_b32 %2, 0xff, %2\n\t"
      "v_cmp_ne_u32 vcc, 0, %2\n\t"
      "v_xor_b32 v132, %6, v132\n\t"
      "v_xor_b32 v133, %7, v133\n\t"
      "v_xor_b32 v134, %8, v134\n\t"
      "v_xor_b32 v135, %9, v135\n\t"
      "s_wqm_b64 exec, vcc\n\t"
      "global_load_dwordx4 v[132:135], %4, %5 offset:512 sc1\n\t"
      "s_mov_b64 exec, -1\n\t"
      "v_or3_b32 %2, v136, v137, v138\n\t"
      "v_or_b32 %2, %2, v139\n\t"
      "v_and_b32 %2, 0xff, %2\n\t"
      "v_cmp_ne_u32 vcc, 0, %2\n\t"
      "v_xor_b32 v136, %6, v136\n\t"
      "v_xor_b32 v137, %7, v137\n\t"
      "v_xor_b32 v138, %8, v138\n\t"
      "v_xor_b32 v139, %9, v139\n\t"
      "s_wqm_b64 exec, vcc\n\t"
      "global_load_dwordx4 v[136:139], %4, %5 offset:1024 sc1\n\t"
      "s_mov_b64 exec, -1\n\t"
      "s_waitcnt vmcnt(0)\n\t"
      "s_branch 1b\n\t"
      "2:\n\t"
      : "+{v[128:135]}"(d0), "+{v[136:139]}"(d1), "=&v"(t), "=&s"(cnt), "+v"(o0)
      : "s"(ws), "s"(tag & 255u), "s"((tag >> 8) & 255u), "s"((tag >> 16) & 255u), "s"(tag >> 24) : "vcc", "scc", "memory");
  return cnt;
}
DEV void issue_o(const unsigned char* ws, unsigned o0, unsigned o1, unsigned xo, v16u& d0, v16u& d1, v4u& x) {
  asm volatile(
      "global_load_dwordx4 v[160:163], %6, %3 sc1\n\t"
      "global_load_dwordx2 v[128:129], %4, %3 offset:-2560 sc1\n\t"
      "global_load_dwordx2 v[130:131], %4, %3 offset:-1920 sc1\n\t"
      "global_load_dwordx2 v[132:133], %4, %3 offset:-1280 sc1\n\t"
      "global_load_dwordx2 v[134:135], %4, %3 offset:-640 sc1\n\t"
      "global_load_dwordx2 v[136:137], %4, %3 offset:0 sc1\n\t"
      "global_load_dwordx2 v[138:139], %4, %3 offset:640 sc1\n\t"
      "global_load_dwordx2 v[140:141], %4, %3 offset:1280 sc1\n\t"
      "global_load_dwordx2 v[142:143], %4, %3 offset:1920 sc1\n\t"
      "global_load_dwordx2 v[144:145], %5, %3 offset:-2560 sc1\n\t"
      "global_load_dwordx2 v[146:147], %5, %3 offset:-1920 sc1\n\t"
      "global_load_dwordx2 v[148:149], %5, %3 offset:-1280 sc1\n\t"
      "global_load_dwordx2 v[150:151], %5, %3 offset:-640 sc1\n\t"
      "global_load_dwordx2 v[152:153], %5, %3 offset:0 sc1\n\t"
      "global_load_dwordx2 v[154:155], %5, %3 offset:640 sc1\n\t"
      "global_load_dwordx2 v[156:157], %5, %3 offset:1280 sc1\n\t"
      "global_load_dwordx2 v[158:159], %5, %3 offset:1920 sc1\n\t"
      : "=&{v[128:143]}"(d0), "=&{v[144:159]}"(d1), "=&{v[160:163]}"(x)
      : "s"(ws), "v"(o0), "v"(o1), "v"(xo) : "memory");
}
DEV unsigned finish_o(const unsigned char* ws, unsigned o0, unsigned o1, unsigned xo, v2u tb, unsigned tag, v16u& d0, v16u& d1, v4u& x) {
  unsigned t, cnt;
  asm volatile(
      "s_mov_b32 %4, 0\n\t"
      "1:\n\t"
      "s_waitcnt vmcnt(15)\n\t"
      "v_xor_b32 v128, %9, v128\n\t"
      "v_xor_b32 v129, %10, v129\n\t"
      "v_or_b32 %3, v128, v129\n\t"
      "s_waitcnt vmcnt(14)\n\t"
      "v_xor_b32 v130, %9, v130\n\t"
      "v_xor_b32 v131, %10, v131\n\t"
      "v_or3_b32 %3, %3, v130, v131\n\t"
      "s_waitcnt vmcnt(13)\n\t"
      "v_xor_b32 v132, %9, v132\n\t"
      "v_xor_b32 v133, %10, v133\n\t"
      "v_or3_b32 %3, %3, v132, v133\n\t"
      "s_waitcnt vmcnt(12)\n\t"
      "v_xor_b32 v134, %9, v134\n\t"
      "v_xor_b32 v135, %10, v135\n\t"
      "v_or3_b32 %3, %3, v134, v135\n\t"
      "s_waitcnt vmcnt(11)\n\t"
      "v_xor_b32 v136, %9, v136\n\t"
      "v_xor_b32 v137, %10, v137\n\t"
      "v_or3_b32 %3, %3, v136, v137\n\t"
      "s_waitcnt vmcnt(10)\n\t"
      "v_xor_b32 v138, %9, v138\n\t"
      "v_xor_b32 v139, %10, v139\n\t"
      "v_or3_b32 %3, %3, v138, v139\n\t"
      "s_waitcnt vmcnt(9)\n\t"
      "v_xor_b32 v140, %9, v140\n\t"
      "v_xor_b32 v141, %10, v141\n\t"
      "v_or3_b32 %3, %3, v140, v141\n\t"
      "s_waitcnt vmcnt(8)\n\t"
      "v_xor_b32 v142, %9, v142\n\t"
      "v_xor_b32 v143, %10, v143\n\t"
      "v_or3_b32 %3, %3, v142, v143\n\t"
      "s_waitcnt vmcnt(7)\n\t"
      "v_xor_b32 v144, %9, v144\n\t"
      "v_xor_b32 v145, %10, v145\n\t"
      "v_or3_b32 %3, %3, v144, v145\n\t"
      "s_waitcnt vmcnt(6)\n\t"
      "v_xor_b32 v146, %9, v146\n\t"
      "v_xor_b32 v147, %10, v147\n\t"
      "v_or3_b32 %3, %3, v146, v147\n\t"
      "s_waitcnt vmcnt(5)\n\t"
      "v_xor_b32 v148, %9, v148\n\t"
      "v_xor_b32 v149, %10, v149\n\t"
      "v_or3_b32 %3, %3, v148, v149\n\t"
      "s_waitcnt vmcnt(4)\n\t"
      "v_xor_b32 v150, %9, v150\n\t"
      "v_xor_b32 v151, %10, v151\n\t"
      "v_or3_b32 %3, %3, v150, v151\n\t"
      "s_waitcnt vmcnt(3)\n\t"
      "v_xor_b32 v152, %9, v152\n\t"
      "v_xor_b32 v153, %10, v153\n\t"
      "v_or3_b32 %3, %3, v152, v153\n\t"
      "s_waitcnt vmcnt(2)\n\t"
      "v_xor_b32 v154, %9, v154\n\t"
      "v_xor_b32 v155, %10, v155\n\t"
      "v_or3_b32 %3, %3, v154, v155\n\t"
      "s_waitcnt vmcnt(1)\n\t"
      "v_xor_b32 v156, %9, v156\n\t"
      "v_xor_b32 v157, %10, v157\n\t"
      "v_or3_b32 %3, %3, v156, v157\n\t"
      "s_waitcnt vmcnt(0)\n\t"
      "v_xor_b32 v158, %9, v158\n\t"
      "v_xor_b32 v159, %10, v159\n\t"
      "v_or3_b32 %3, %3, v158, v159\n\t"
      "v_and_b32 %3, 0xff, %3\n\t"
      "v_bitop3_b32 %3, %3, %11, v161 bitop3:0xf6\n\t"
      "v_bitop3_b32 %3, %3, %11, v163 bitop3:0xf6\n\t"
      "v_cmp_ne_u32 vcc, 0, %3\n\t"
      "s_cbranch_vccz 2f\n\t"
      "s_add_u32 %4, %4, 1\n\t"
      "s_bitcmp1_b32 %4, 20\n\t"
      "s_cbranch_scc1 2f\n\t"
      "s_sleep 2\n\t"
      "v_xor_b32 %3, %11, v161\n\t"
      "v_bitop3_b32 %3, %3, %11, v163 bitop3:0xf6\n\t"
      "v_cmp_ne_u32 vcc, 0, %3\n\t"
      "s_mov_b64 exec, vcc\n\t"
      "global_load_dwordx4 v[160:163], %7, %8 sc1\n\t"
      "s_mov_b64 exec, -1\n\t"
      "v_or_b32 %3, v128, v129\n\t"
      "v_and_b32 %3, 0xff, %3\n\t"
      "v_cmp_ne_u32 vcc, 0, %3\n\t"
      "v_xor_b32 v128, %9, v128\n\t"
      "v_xor_b32 v129, %10, v129\n\t"
      "s_wqm_b64 exec, vcc\n\t"
      "global_load_dwordx2 v[128:129], %5, %8 offset:-2560 sc1\n\t"
      "s_mov_b64 exec, -1\n\t"
      "v_or_b32 %3, v130, v131\n\t"
      "v_and_b32 %3, 0xff, %3\n\t"
      "v_cmp_ne_u32 vcc, 0, %3\n\t"
      "v_xor_b32 v130, %9, v130\n\t"
      "v_xor_b32 v131, %10, v131\n\t"
      "s_wqm_b64 exec, vcc\n\t"
      "global_load_dwordx2 v[130:131], %5, %8 offset:-1920 sc1\n\t"
      "s_mov_b64 exec, -1\n\t"
      "v_or_b32 %3, v132, v133\n\t"
      "v_and_b32 %3, 0xff, %3\n\t"
      "v_cmp_ne_u32 vcc, 0, %3\n\t"
      "v_xor_b32 v132, %9, v132\n\t"
      "v_xor_b32 v133, %10, v133\n\t"
      "s_wqm_b64 exec, vcc\n\t"
      "global_load_dwordx2 v[132:133], %5, %8 offset:-1280 sc1\n\t"
      "s_mov_b64 exec, -1\n\t"
      "v_or_b32 %3, v134, v135\n\t"
      "v_and_b32 %3, 0xff, %3\n\t"
      "v_cmp_ne_u32 vcc, 0, %3\n\t"
      "v_xor_b32 v134, %9, v134\n\t"
      "v_xor_b32 v135, %10, v135\n\t"
      "s_wqm_b64 exec, vcc\n\t"
      "global_load_dwordx2 v[134:135], %5, %8 offset:-640 sc1\n\t"
      "s_mov_b64 exec, -1\n\t"
      "v_or_b32 %3, v136, v137\n\t"
      "v_and_b32 %3, 0xff, %3\n\t"
      "v_cmp_ne_u32 vcc, 0, %3\n\t"
      "v_xor_b32 v136, %9, v136\n\t"
      "v_xor_b32 v137, %10, v137\n\t"
      "s_wqm_b64 exec, vcc\n\t"
      "global_load_dwordx2 v[136:137], %5, %8 offset:0 sc1\n\t"
      "s_mov_b64 exec, -1\n\t"
      "v_or_b32 %3, v138, v139\n\t"
      "v_and_b32 %3, 0xff, %3\n\t"
      "v_cmp_ne_u32 vcc, 0, %3\n\t"
      "v_xor_b32 v138, %9, v138\n\t"
      "v_xor_b32 v139, %10, v139\n\t"
      "s_wqm_b64 exec, vcc\n\t"
      "global_load_dwordx2 v[138:139], %5, %8 offset:640 sc1\n\t"
      "s_mov_b64 exec, -1\n\t"
      "v_or_b32 %3, v140, v141\n\t"
      "v_and_b32 %3, 0xff, %3\n\t"
      "v_cmp_ne_u32 vcc, 0, %3\n\t"
      "v_xor_b32 v140, %9, v140\n\t"
      "v_xor_b32 v141, %10, v141\n\t"
      "s_wqm_b64 exec, vcc\n\t"
      "global_load_dwordx2 v[140:141], %5, %8 offset:1280 sc1\n\t"
      "s_mov_b64 exec, -1\n\t"
      "v_or_b32 %3, v142, v143\n\t"
      "v_and_b32 %3, 0xff, %3\n\t"
      "v_cmp_ne_u32 vcc, 0, %3\n\t"
      "v_xor_b32 v142, %9, v142\n\t"
      "v_xor_b32 v143, %10, v143\n\t"
      "s_wqm_b64 exec, vcc\n\t"
      "global_load_dwordx2 v[142:143], %5, %8 offset:1920 sc1\n\t"
      "s_mov_b64 exec, -1\n\t"
      "v_or_b32 %3, v144, v145\n\t"
      "v_and_b32 %3, 0xff, %3\n\t"
      "v_cmp_ne_u32 vcc, 0, %3\n\t"
      "v_xor_b32 v144, %9, v144\n\t"
      "v_xor_b32 v145, %10, v145\n\t"
      "s_wqm_b64 exec, vcc\n\t"
      "global_load_dwordx2 v[144:145], %6, %8 offset:-2560 sc1\n\t"
      "s_mov_b64 exec, -1\n\t"
      "v_or_b32 %3, v146, v147\n\t"
      "v_and_b32 %3, 0xff, %3\n\t"
      "v_cmp_ne_u32 vcc, 0, %3\n\t"
      "v_xor_b32 v146, %9, v146\n\t"
      "v_xor_b32 v147, %10, v147\n\t"
      "s_wqm_b64 exec, vcc\n\t"
      "global_load_dwordx2 v[146:147], %6, %8 offset:-1920 sc1\n\t"
      "s_mov_b64 exec, -1\n\t"
      "v_or_b32 %3, v148, v149\n\t"
      "v_and_b32 %3, 0xff, %3\n\t"
      "v_cmp_ne_u32 vcc, 0, %3\n\t"
      "v_xor_b32 v148, %9, v148\n\t"
      "v_xor_b32 v149, %10, v149\n\t"
      "s_wqm_b64 exec, vcc\n\t"
      "global_load_dwordx2 v[148:149], %6, %8 offset:-1280 sc1\n\t"
      "s_mov_b64 exec, -1\n\t"
      "v_or_b32 %3, v150, v151\n\t"
      "v_and_b32 %3, 0xff, %3\n\t"
      "v_cmp_ne_u32 vcc, 0, %3\n\t"
      "v_xor_b32 v150, %9, v150\n\t"
      "v_xor_b32 v151, %10, v151\n\t"
      "s_wqm_b64 exec, vcc\n\t"
      "global_load_dwordx2 v[150:151], %6, %8 offset:-640 sc1\n\t"
      "s_mov_b64 exec, -1\n\t"
      "v_or_b32 %3, v152, v153\n\t"
      "v_and_b32 %3, 0xff, %3\n\t"
      "v_cmp_ne_u32 vcc, 0, %3\n\t"
      "v_xor_b32 v152, %9, v152\n\t"
      "v_xor_b32 v153, %10, v153\n\t"
      "s_wqm_b64 exec, vcc\n\t"
      "global_load_dwordx2 v[152:153], %6, %8 offset:0 sc1\n\t"
      "s_mov_b64 exec, -1\n\t"
      "v_or_b32 %3, v154, v155\n\t"
      "v_and_b32 %3, 0xff, %3\n\t"
      "v_cmp_ne_u32 vcc, 0, %3\n\t"
      "v_xor_b32 v154, %9, v154\n\t"
      "v_xor_b32 v155, %10, v155\n\t"
      "s_wqm_b64 exec, vcc\n\t"
      "global_load_dwordx2 v[154:155], %6, %8 offset:640 sc1\n\t"
      "s_mov_b64 exec, -1\n\t"
      "v_or_b32 %3, v156, v157\n\t"
      "v_and_b32 %3, 0xff, %3\n\t"
      "v_cmp_ne_u32 vcc, 0, %3\n\t"
      "v_xor_b32 v156, %9, v156\n\t"
      "v_xor_b32 v157, %10, v157\n\t"
      "s_wqm_b64 exec, vcc\n\t"
      "global_load_dwordx2 v[156:157], %6, %8 offset:1280 sc1\n\t"
      "s_mov_b64 exec, -1\n\t"
      "v_or_b32 %3, v158, v159\n\t"
      "v_and_b32 %3, 0xff, %3\n\t"
      "v_cmp_ne_u32 vcc, 0, %3\n\t"
      "v_xor_b32 v158, %9, v158\n\t"
      "v_xor_b32 v159, %10, v159\n\t"
      "s_wqm_b64 exec, vcc\n\t"
      "global_load_dwordx2 v[158:159], %6, %8 offset:1920 sc1\n\t"
      "s_mov_b64 exec, -1\n\t"
      "s_waitcnt vmcnt(0)\n\t"
      "s_branch 1b\n\t"
      "2:\n\t"
      : "+{v[128:143]}"(d0), "+{v[144:159]}"(d1), "+{v[160:163]}"(x), "=&v"(t), "=&s"(cnt), "+v"(o0), "+v"(o1), "+v"(xo)
      : "s"(ws), "v"(tb.x), "v"(tb.y), "s"(tag) : "vcc", "scc", "memory");
  return cnt;
}
DEV void issue_f(const unsigned char* ws, unsigned o0, v8u& d0) {
  asm volatile(
      "global_load_dword v128, %2, %1 offset:0 sc1\n\t"
      "global_load_dword v129, %2, %1 offset:512 sc1\n\t"
      "global_load_dword v130, %2, %1 offset:1024 sc1\n\t"
      "global_load_dword v131, %2, %1 offset:1536 sc1\n\t"
      "global_load_dword v132, %2, %1 offset:2048 sc1\n\t"
      "global_load_dword v133, %2, %1 offset:2560 sc1\n\t"
      "global_load_dword v134, %2, %1 offset:3072 sc1\n\t"
      "global_load_dword v135, %2, %1 offset:3584 sc1\n\t"
      : "=&{v[128:135]}"(d0)
      : "s"(ws), "v"(o0) : "memory");
}
DEV unsigned finish_f(const unsigned char* ws, unsigned o0, unsigned tb, v8u& d0) {
  unsigned t, cnt;
  asm volatile(
      "s_mov_b32 %2, 0\n\t"
      "1:\n\t"
      "s_waitcnt vmcnt(0)\n\t"
      "v_xor_b32 v128, %5, v128\n\t"
      "v_xor_b32 v129, %5, v129\n\t"
      "v_xor_b32 v130, %5, v130\n\t"
      "v_xor_b32 v131, %5, v131\n\t"
      "v_xor_b32 v132, %5, v132\n\t"
      "v_xor_b32 v133, %5, v133\n\t"
      "v_xor_b32 v134, %5, v134\n\t"
      "v_xor_b32 v135, %5, v135\n\t"
      "v_or_b32 %1, v128, v129\n\t"
      "v_or3_b32 %1, %1, v130, v131\n\t"
      "v_or3_b32 %1, %1, v132, v133\n\t"
      "v_or3_b32 %1, %1, v134, v135\n\t"
      "v_and_b32 %1, 0xff, %1\n\t"
      "v_cmp_ne_u32 vcc, 0, %1\n\t"
      "s_cbranch_vccz 2f\n\t"
      "s_add_u32 %2, %2, 1\n\t"
      "s_bitcmp1_b32 %2, 20\n\t"
      "s_cbranch_scc1 2f\n\t"
      "v_and_b32 %1, 0xff, v128\n\t"
      "v_cmp_ne_u32 vcc, 0, %1\n\t"
      "v_xor_b32 v128, %5, v128\n\t"
      "s_cbranch_vccz 4f\n\t"
      "global_load_dword v128, %3, %4 offset:0 sc1\n\t"
      "4:\n\t"
      "v_and_b32 %1, 0xff, v129\n\t"
      "v_cmp_ne_u32 vcc, 0, %1\n\t"
      "v_xor_b32 v129, %5, v129\n\t"
      "s_cbranch_vccz 4f\n\t"
      "global_load_dword v129, %3, %4 offset:512 sc1\n\t"
      "4:\n\t"
      "v_and_b32 %1, 0xff, v130\n\t"
      "v_cmp_ne_u32 vcc, 0, %1\n\t"
      "v_xor_b32 v130, %5, v130\n\t"
      "s_cbranch_vccz 4f\n\t"
      "global_load_dword v130, %3, %4 offset:1024 sc1\n\t"
      "4:\n\t"
      "v_and_b32 %1, 0xff, v131\n\t"
      "v_cmp_ne_u32 vcc, 0, %1\n\t"
      "v_xor_b32 v131, %5, v131\n\t"
      "s_cbranch_vccz 4f\n\t"
      "global_load_dword v131, %3, %4 offset:1536 sc1\n\t"
      "4:\n\t"
      "v_and_b32 %1, 0xff, v132\n\t"
      "v_cmp_ne_u32 vcc, 0, %1\n\t"
      "v_xor_b32 v132, %5, v132\n\t"
      "s_cbranch_vccz 4f\n\t"
      "global_load_dword v132, %3, %4 offset:2048 sc1\n\t"
      "4:\n\t"
      "v_and_b32 %1, 0xff, v133\n\t"
      "v_cmp_ne_u32 vcc, 0, %1\n\t"
      "v_xor_b32 v133, %5, v133\n\t"
      "s_cbranch_vccz 4f\n\t"
      "global_load_dword v133, %3, %4 offset:2560 sc1\n\t"
      "4:\n\t"
      "v_and_b32 %1, 0xff, v134\n\t"
      "v_cmp_ne_u32 vcc, 0, %1\n\t"
      "v_xor_b32 v134, %5, v134\n\t"
      "s_cbranch_vccz 4f\n\t"
      "global_load_dword v134, %3, %4 offset:3072 sc1\n\t"
      "4:\n\t"
      "v_and_b32 %1, 0xff, v135\n\t"
      "v_cmp_ne_u32 vcc, 0, %1\n\t"
      "v_xor_b32 v135, %5, v135\n\t"
      "s_cbranch_vccz 4f\n\t"
      "global_load_dword v135, %3, %4 offset:3584 sc1\n\t"
      "4:\n\t"
      "s_waitcnt vmcnt(0)\n\t"
      "s_branch 1b\n\t"
      "2:\n\t"
      : "+{v[128:135]}"(d0), "=&v"(t), "=&s"(cnt), "+v"(o0)
      : "s"(ws), "v"(tb) : "vcc", "scc", "memory");
  return cnt;
}

DEV v4u trpair(lptr L, int a) {
  const v2u t0 = __builtin_bit_cast(v2u, __builtin_amdgcn_ds_read_tr16_b64_v4i16((LAS s4*)(L + a)));
  const v2u t1 = __builtin_bit_cast(v2u, __builtin_amdgcn_ds_read_tr16_b64_v4i16((LAS s4*)(L + a + 4096)));
  return v4u{t0.x, t0.y, t1.x, t1.y};
}
DEV f4 mfma32(v4u a, v4u b, f4 c) { return __builtin_amdgcn_mfma_f32_16x16x32_bf16(__builtin_bit_cast(b8, a), __builtin_bit_cast(b8, b), c, 0, 0, 0); }
template <int NCB, int NKS> DEV void gemv(lptr L, const int (&ad)[NCB], int toff, const v4u* xf, f4 (&acc)[NCB]) {
  v4u wf[NKS][NCB];
#pragma unroll
  for (int ks = 0; ks < NKS; ++ks)
#pragma unroll
    for (int i = 0; i < NCB; ++i) wf[ks][i] = trpair(L, ad[i] + toff + 8192 * ks);
#pragma unroll
  for (int ks = 0; ks < NKS; ++ks)
#pragma unroll
    for (int i = 0; i < NCB; ++i) acc[i] = mfma32(wf[ks][i], xf[ks], acc[i]);
}
DEV int fragpos(int e) { return 64 * (e >> 5) + 16 * ((e >> 2) & 3) + 8 * ((e >> 4) & 1) + 2 * (e & 3); }

#define SEG_TAIL "s_waitcnt vmcnt(%8)\n\tv_add_u32 %2, 4, %2\n\ts_sub_u32 %4, %4, 1\n\tv_max_i32 %3, %3, %2\n\ts_cmp_lg_u32 %4, 0\n\tds_write_b32 %7, %3\n\ts_cbranch_scc1 1b"
#define SEG_PUB "v_add_u32 %2, 4, %2\n\tv_max_i32 %3, %3, %2\n\tds_write_b32 %7, %3\n\t"
#define SEG_ROWS(MID, END) \
  asm volatile("s_mov_b32 m0, %6\n1:\n\t" \
               "v_add_u32 %1, %9, %0\n\tglobal_load_lds_dwordx4 %0, %5" NT_AUX "\n\ts_add_u32 m0, m0, 0x800\n\tv_add_u32 %0, %9, %1\n\t" \
               "global_load_lds_dwordx4 %1, %5" NT_AUX "\n\ts_add_u32 m0, m0, 0x800\n\tv_add_u32 %1, %9, %0\n\ts_waitcnt vmcnt(%8)\n\t" MID \
               "global_load_lds_dwordx4 %0, %5" NT_AUX "\n\ts_add_u32 m0, m0, 0x800\n\tv_add_u32 %0, %9, %1\n\t" \
               "global_load_lds_dwordx4 %1, %5" NT_AUX "\n\ts_add_u32 m0, m0, 0x800\n\ts_waitcnt vmcnt(%8)\n\t" END \
               "s_sub_u32 %4, %4, 1\n\ts_cmp_lg_u32 %4, 0\n\ts_cbranch_scc1 1b" \
               : "+&v"(voff), "=&v"(t), "+&v"(landed), "+&v"(pub), "+&s"(nq) : "s"(base), "s"(m), "v"(fl), "n"(WIN - 2), "s"(step) : "scc", "memory")
template <int WIN> DEV void seg_rows(unsigned fl, const void* base, unsigned voff, unsigned step, unsigned m, int nq, int landed, int& pub) {
  unsigned t;
  if constexpr (WIN % 4 == 2) { SEG_ROWS("", SEG_PUB); } else { landed -= 2; SEG_ROWS(SEG_PUB, ""); }
}
template <int WIN> DEV void seg_flat(unsigned fl, const void* base, unsigned voff, unsigned m, int nq, int landed, int& pub) {
  unsigned t;
  voff += 4096;
  asm volatile("s_mov_b32 m0, %6\n\ts_nop 0\n1:\n\t"
               "global_load_lds_dwordx4 %0, %5 offset:-4096" NT_AUX "\n\tglobal_load_lds_dwordx4 %0, %5 offset:-2048" NT_AUX "\n\ts_waitcnt vmcnt(%8)\n\t"
               "global_load_lds_dwordx4 %0, %5" NT_AUX "\n\tglobal_load_lds_dwordx4 %0, %5 offset:2048" NT_AUX "\n\t"
               "s_add_u32 m0, m0, 0x2000\n\tv_add_u32 %0, 0x2000, %0\n\t" SEG_TAIL
               : "+&v"(voff), "=&v"(t), "+&v"(landed), "+&v"(pub), "+&s"(nq) : "s"(base), "s"(m + 4096), "v"(fl), "n"(WIN - 2) : "scc", "memory");
}
template <int HI, int LO, int STEP> DEV void drain(lptr L, int j, int issued, int& pub) {
  if constexpr (HI >= LO) { waitvm<HI>(); pub = pub > issued - HI ? pub : issued - HI; *flg(L, j) = pub; drain<HI - STEP, LO, STEP>(L, j, issued, pub); }
}

__global__ void __launch_bounds__(640, 3)
fused_decode(const u16*, const u16*, const u16*, unsigned char*, const u16*, const u16*, const u16*, const u16*, const u16*, u16*) {
  extern __shared__ __attribute__((aligned(16))) unsigned char smem[];
  lptr L = (lptr)smem;
  int c = blockIdx.x, tid = threadIdx.x;
  const unsigned lb = (unsigned)(size_t)L;
  u64 px, pwq, pkc, pws, pvc, pwg, pwu, pwo, pwd, pout, tk;
  unsigned tag;
  {
    unsigned v0, v1, v2;
    asm volatile(
        "s_load_dwordx16 s[64:79], %[ka], 0x0\n\tv_readfirstlane_b32 s84, %[tid]\n\tv_lshlrev_b32 %[v1], 4, %[tid]\n\t"
        "s_cmp_lt_u32 s84, 0x80\n\ts_cbranch_scc1 3f\n\t"
        "s_getpc_b64 s[84:85]\n\tv_lshl_add_u32 %[v0], %[tid], 7, %[m16k]\n\ts_add_u32 m0, %[lb], %[dum]\n\t"
        "v_cmp_gt_u32 vcc, %[cb], %[v0]\n\ts_cbranch_vccz 1f\n\t"
        "s_mov_b64 exec, vcc\n\tglobal_load_lds_dword %[v0], s[84:85]\n\ts_mov_b64 exec, -1\n"
        "1:\n\ts_load_dwordx4 s[80:83], %[ka], 0x40\n\tv_and_b32 %[v0], 31, %[tid]\n\tv_lshl_add_u32 %[v0], %[v0], 2, m0\n\t"
        "v_mov_b32 %[v1], 0\n\tv_add_u32 %[v0], 0xffffff80, %[v0]\n\tds_write_b32 %[v0], %[v1]\n\ts_branch 2f\n"
        "3:\n\ts_setprio 3\n\tv_lshrrev_b32 %[v0], 4, %[tid]\n\ts_lshl_b32 s84, s84, 4\n\tv_and_b32 %[v2], 0xf0, %[v1]\n\ts_add_u32 s84, s84, %[lb]\n\t"
        "s_cmp_lt_u32 %[c], 64\n\ts_cbranch_scc1 4f\n\t"
        "s_lshr_b32 s85, %[c], 5\n\ts_mul_i32 s86, s85, 5\n\ts_cmp_eq_u32 s85, 7\n\ts_addc_u32 s86, s86, -10\n\ts_lshl_b32 s86, s86, 10\n\t"
        "s_xor_b32 s85, %[c], 1\n\ts_and_b32 s85, s85, 31\n\ts_or_b32 s86, s86, s85\n\ts_lshl_b32 s88, s86, 8\n\t"
        "v_mul_u32_u24 %[v0], 0x2020, %[v0]\n\ts_mov_b32 s85, 0x10000\n\ts_add_u32 m0, s84, %[xr]\n\tv_xor_b32 %[v2], %[v2], %[v0]\n\t"
        "s_waitcnt lgkmcnt(0)\n\tglobal_load_lds_dwordx4 %[v1], s[64:65]\n\ts_mov_b64 s[64:65], s[66:67]\n\tv_add_u32 %[v2], s88, %[v2]\n\ts_branch 5f\n"
        "4:\n\ts_and_b32 s86, %[c], 7\n\ts_mul_i32 s86, s86, 72\n\ts_and_b32 s86, s86, 0x188\n\ts_lshr_b32 s85, %[c], 3\n\ts_or_b32 s86, s86, s85\n\ts_or_b32 s86, s86, 0x70\n\ts_lshl_b32 s88, s86, 16\n\t"
        "v_mul_u32_u24 %[v0], 0x120, %[v0]\n\ts_movk_i32 s85, 0x800\n\ts_add_u32 m0, s84, %[xr]\n\tv_xor_b32 %[v2], %[v2], %[v0]\n\t"
        "s_bitcmp1_b32 s84, 10\n\ts_waitcnt lgkmcnt(0)\n\tglobal_load_lds_dwordx4 %[v1], s[64:65]\n\ts_mov_b64 s[64:65], s[68:69]\n\tv_add_u32 %[v2], s88, %[v2]\n\t"
        "s_cbranch_scc0 5f\n\ts_load_dword s88, s[72:73], s88\n"
        "5:\n\ts_mov_b32 m0, s84\n\ts_add_u32 s84, s84, 0xa000\n\ts_memrealtime s[86:87]\n\ts_load_dwordx4 s[80:83], %[ka], 0x40\n"
        "6:\n\tv_add_u32 %[v1], s85, %[v2]\n\tglobal_load_lds_dwordx4 %[v2], s[64:65]" NT_AUX "\n\ts_add_u32 m0, m0, 0x800\n\tv_add_u32 %[v2], s85, %[v1]\n\t"
        "global_load_lds_dwordx4 %[v1], s[64:65]" NT_AUX "\n\ts_add_u32 m0, m0, 0x800\n\ts_cmp_lt_u32 m0, s84\n\ts_cbranch_scc1 6b\n"
        "2:"
        : [v0] "=&v"(v0), [v1] "=&v"(v1), [v2] "=&v"(v2), [c] "+s"(c), [tid] "+v"(tid), "=&{s[64:65]}"(px), "=&{s[66:67]}"(pwq), "=&{s[68:69]}"(pkc),
          "=&{s[70:71]}"(pws), "=&{s[72:73]}"(pvc), "=&{s[74:75]}"(pwg), "=&{s[76:77]}"(pwu), "=&{s[78:79]}"(pwo),
          "=&{s[80:81]}"(pwd), "=&{s[82:83]}"(pout), "=&{s[86:87]}"(tk), "=&{s88}"(tag)
        : [ka] "s"(__builtin_amdgcn_kernarg_segment_ptr()), [cb] "n"(CODE_BYTES), [lb] "s"(lb), [dum] "n"(L_DUM), [xr] "n"(L_XR - T0), [m16k] "s"(-16384)
        : "s84", "s85", "vcc", "scc", "memory");
    static_assert(L_DUM == L_FLG + 0x80 && T0 == 0, "entry code");
  }
  const int lane = tid & 63, wave = __builtin_amdgcn_readfirstlane(tid >> 6);
  const bool isA = c < 64;
  const int g = (c >> 3) & 3, hh = c >> 5, cb = (c + hh) & 7, h = c & 31;
  const int ga = (c >> 1) & 3, sp = (c & 1) * 8 + (c >> 3);
  const int hq = (c ^ 1) & 31, kq0 = 5 * (hh - 2) + ((hh + 1) >> 3), nkq = hh >= 6 ? 6 : 5;
  const unsigned kvo = (ga * SK + (SK - WKEYS) + sp * 256) * 256;
  const int b = (hh - 2) * 8 + ((c + 2 * g) & 7), part = (b * 37) >> 9, strip = b - 14 * part;
  const int nk = isA || b >= 42 ? 0 : (part == 2 ? 10 : 11);
  const int kd0 = isA ? 4 * (cb < 6 ? cb : 6) + 2 * hh : 4 * ((c & 7) < 7 ? c & 7 : hh - 2), nkd = isA ? 2 : 4;
  const int n_gu = 4 * nkq, n_wo = isA ? 64 : n_gu + 4 * nk, n_wd = n_wo + 16, n_all = n_wd + 4 * nkd;
  const int wd_reg = isA ? T1 : TG;
  const unsigned fl1 = lb + L_FLG, fl2 = fl1 + 4 * (lane & 1);
  const unsigned r8 = tid >> 4;
  const unsigned sw = ((lane & 15) ^ (r8 << 1)) << 4;
  {
    const unsigned need = 8 + 2 * ((unsigned)(c - 64) >> 31);
    unsigned v0;
    asm volatile("" :: "s"(n_all), "s"(n_wo), "s"(kd0), "s"(part), "s"(b), "s"(h), "v"(fl2), "v"(sw));
    asm volatile("s_cmp_gt_u32 %[wave], 1\n\ts_cbranch_scc0 3f\n\ts_cmp_ge_u32 %[wave], %[need]\n\ts_waitcnt lgkmcnt(0)\n\ts_cbranch_scc1 4f\n\t"
                 "s_load_dword s88, s[70:71], 0x0\n4:\n\ts_barrier\n\ts_waitcnt lgkmcnt(0)\n\ts_branch 2f\n"
                 "3:\n\tv_mov_b32 %[v0], 1\n\ts_cmp_lg_u32 %[wave], 0\n\ts_cbranch_scc1 5f\n\ts_waitcnt lgkmcnt(0)\n\t"
                 "s_and_b32 s86, s86, 0x3fffffff\n\tv_or_b32 %[v0], s86, %[v0]\n"
                 "5:\n\ts_barrier\n\ts_waitcnt vmcnt(20)\n\tds_write_b32 %[xf], %[v0]\n"
                 "2:"
                 : [v0] "=&v"(v0), "+{s88}"(tag), "+{s[66:67]}"(pwq), "+{s[68:69]}"(pkc), "+{s[70:71]}"(pws), "+{s[86:87]}"(tk),
                   "+{s[72:73]}"(pvc), "+{s[74:75]}"(pwg), "+{s[76:77]}"(pwu), "+{s[78:79]}"(pwo)
                 : [wave] "s"(wave), [xf] "v"(fl1 + (F_XR0 + wave) * 4), [need] "s"(need), "{s[64:65]}"(px) : "scc", "memory");
  }
  const char *wq = (const char*)pwq, *kc = (const char*)pkc, *vc = (const char*)pvc, *wg = (const char*)pwg, *wu = (const char*)pwu,
             *wo = (const char*)pwo;
  unsigned char* ws = (unsigned char*)pws;

  if (wave < 2) {
    const int j = wave;
    int pub = 0;
    const unsigned fl = fl1 + 4 * j, mb = lb + j * 1024;
    if (isA) {
      seg_flat<WIN_KV>(fl, kc, kvo + sw + r8 * 256 + 20 * 2048, mb + T0 + 20 * 2048, 3, 20 - (WIN_KV - 2), pub);
      seg_flat<WIN_KV>(fl, vc, kvo + sw + r8 * 256, mb + T2, 8, 32 - (WIN_KV - 2), pub);
      drain<QD, QD, 4>(L, j, 64, pub);
      drain<0, 0, 4>(L, j, 64, pub);
      wait_flag(F_QC, 4);
    } else {
      if (nkq == 6) seg_rows<22>(fl, wq, sw + (r8 + kq0 * 32 + 160) * 8192 + hq * 256, 8 * 8192, mb + T0 + 20 * 2048, 1, 0, pub);
      if (nk) {
        const char* bg = (const char*)(strip >= 7 ? wu : wg) + (size_t)part * 11 * 32 * 7168 + (7 * g + (strip >= 7 ? strip - 7 : strip)) * 256;
        seg_rows<WIN_G1>(fl, bg, sw + r8 * 7168, 8 * 7168, mb + TG, NG1, n_gu - (WIN_G1 - 2), pub);
        seg_rows<WIN_G2>(fl, bg, sw + (r8 + 32 * NG1) * 7168, 8 * 7168, mb + TG + NG1 * 8192, nk - NG1, n_gu + 4 * NG1 - (WIN_G2 - 2), pub);
      } else {
        drain<4, 0, 4>(L, j, n_gu, pub);
      }
      wait_flag(F_WQD, 4);
      if (nk == 0) { for (int i = 0; i < IDLY; ++i) __builtin_amdgcn_s_sleep(16); }
    }
    seg_rows<WIN_WO>(fl, (const char*)wo + (size_t)h * 128 * 2048 + cb * 256, sw + r8 * 2048, 8 * 2048, mb + T0, 4, n_wo - (WIN_WO - 2), pub);
    if (nk) wait_flag(F_GU4, 4);
    asm volatile("s_waitcnt lgkmcnt(0)" : "+{s[80:81]}"(pwd), "+{s[82:83]}"(pout), "+{s88}"(tag), "+{s[86:87]}"(tk));
    seg_rows<WIN_WD>(fl, (const char*)pwd + (size_t)(28 * g + kd0) * 32 * 2048 + cb * 256, sw + r8 * 2048, 8 * 2048, mb + wd_reg, nkd, n_wd - (WIN_WD - 2), pub);
    drain<12, 0, 4>(L, j, n_all, pub);
    asm volatile("s_endpgm" ::: "memory");
  }

  const int w = wave - 2;
  asm volatile("" : "+{s[80:81]}"(pwd), "+{s[82:83]}"(pout));
  const char *wd = (const char*)pwd, *out = (const char*)pout;
  tag += 0x9E3779B1u;
  inc_flag(L, F_EP);
  const int n16 = lane & 15, g4 = lane >> 4;
  const int trq = (lane >> 2) & 3, rr = 4 * (g4 & 1) + trq;
  const int trbase = 256 * (4 * g4 + trq) + 8 * (lane & 3);

  if (w == 7) {
    if (!isA) {
      wait_lds<0, F_XR0 * 4>(fl2, 1);
      const v4u xa = *(LAS v4u*)(L + L_XR + lane * 32), xb = *(LAS v4u*)(L + L_XR + lane * 32 + 16);
      float v[16];
#pragma unroll
      for (int k = 0; k < 4; ++k) { v[2 * k] = bf_lo(xa[k]); v[2 * k + 1] = bf_hi(xa[k]); v[8 + 2 * k] = bf_lo(xb[k]); v[9 + 2 * k] = bf_hi(xb[k]); }
      float s4_[4];
#pragma unroll
      for (int k = 0; k < 4; ++k) s4_[k] = (v[k] + v[4 + k]) + (v[8 + k] + v[12 + k]);
      const float mean = wave_sum((s4_[0] + s4_[1]) + (s4_[2] + s4_[3])) * (1.f / 1024.f);
#pragma unroll
      for (int k = 0; k < 4; ++k) {
        const float d0 = v[k] - mean, d1 = v[4 + k] - mean, d2 = v[8 + k] - mean, d3 = v[12 + k] - mean;
        s4_[k] = fmaf(d0, d0, d1 * d1) + fmaf(d2, d2, d3 * d3);
      }
      const float var = wave_sum((s4_[0] + s4_[1]) + (s4_[2] + s4_[3])) * (1.f / 1024.f);
      const float mb = rbf(mean), rden = __builtin_amdgcn_rcpf(rbf(__builtin_amdgcn_sqrtf(rbf(rbf(var) + 1e-5f))));
#pragma unroll
      for (int k = 0; k < 4; ++k) {
        const unsigned a = pko(v[4 * k] - mb, v[4 * k + 1] - mb), b_ = pko(v[4 * k + 2] - mb, v[4 * k + 3] - mb);
        *(LAS v2u*)(L + L_XN + fragpos(lane * 16) + 16 * k) = v2u{pk_bf16(bf_lo(a) * rden, bf_hi(a) * rden), pk_bf16(bf_lo(b_) * rden, bf_hi(b_) * rden)};
      }
      set_flag(L, F_XN, 1);
    }
  }
  unsigned t0 = 0;
  if (w >= 4) { wait_flag(F_XR0, 1); t0 = __builtin_amdgcn_readfirstlane(*flg(L, F_XR0)); }
  if (w == 6) {
    const char* bo = wo + (size_t)h * 128 * 2048;
    const char* bg = nk ? (strip >= 7 ? wu : wg) + (size_t)part * 11 * 32 * 7168 : bo;
    const unsigned st = nk ? 106 * 7168 : 0;
    unsigned d0, d1, d2, d3, d4, d5, d6;
    wait_until(t0, T_T);
    asm volatile("s_load_dword %0, %7, 0x0\n\ts_load_dword %1, %7, %11\n\ts_load_dword %2, %7, %12\n\ts_load_dword %3, %7, %13\n\t"
                 "s_load_dword %4, %8, 0x0\n\ts_load_dword %5, %9, 0x0\n\ts_load_dword %6, %10, 0x0\n\ts_waitcnt lgkmcnt(0)"
                 : "=&s"(d0), "=&s"(d1), "=&s"(d2), "=&s"(d3), "=&s"(d4), "=&s"(d5), "=&s"(d6)
                 : "s"(bg), "s"(bo), "s"(wd + (size_t)(28 * g + kd0) * 32 * 2048), "s"(out), "s"(st), "s"(2 * st), "s"(3 * st) : "memory");
  }
  int ad[2];
  ad[0] = trbase + (((2 * (w & 3)) ^ rr) << 5); ad[1] = trbase + (((2 * (w & 3) + 1) ^ rr) << 5);

  if (w < 4) {
    const v4u tb = tagbytes(tag);
    if (!isA) {
      wait_flag(F_XN, 1);
      wait_flag(F_EP, 8);
      const int npass = nk ? 2 : 1;
#pragma nounroll
      for (int pass = 0; pass < npass; ++pass) {
        unsigned po = (pass ? WS_GU + (g * 42 + 3 * strip + part) * 512 : WS_QP + (hq * 6 + hh - 2) * 512) + ((2 * w + n16) * 16 + g4 * 4) * 4;
        PIN(po);
        int xa = L_XN + (pass ? 11 * part : kq0) * 64 + g4 * 16;
        int a2[2] = {ad[0] + (pass ? TG : T0), ad[1] + (pass ? TG : T0)};
        int thr = pass ? n_gu : 0, left = pass ? nk : nkq;
        f4 acc[2] = {f4{0.f, 0.f, 0.f, 0.f}, f4{0.f, 0.f, 0.f, 0.f}};
#pragma nounroll
        for (; left > 4; left -= 4) {
          v4u xf[4];
#pragma unroll
          for (int ks = 0; ks < 4; ++ks) xf[ks] = *(LAS v4u*)(L + xa + ks * 64);
          thr += 16;
          wait_land(thr);
          gemv<2, 4>(L, a2, 0, xf, acc);
          if (left > 8) inc_flag(L, F_GU4);
          xa += 256; a2[0] += 32768; a2[1] += 32768;
        }
#pragma nounroll
        for (; left > 0; --left) {
          const v4u xf = *(LAS v4u*)(L + xa);
          thr += 4;
          wait_land0(thr);
          gemv<2, 1>(L, a2, 0, &xf, acc);
          xa += 64; a2[0] += 8192; a2[1] += 8192;
        }
        const f4 r = (n16 & 1) ? acc[1] : acc[0];
        st16m(ws, po, enc4(r, tb), 0x0003000300030003ull);
        inc_flag(L, F_WQD);
      }
    } else {
      const int hd = n16 & 7;
      const bool hi = n16 >= 8;
      int pa = L_P + (g4 * 8 + hd) * 16, mla = L_ML + hd * 32;
      {
        v4u kf[16];
        wait_land(8 * (w + 1));
#pragma unroll
        for (int i = 0; i < 16; ++i) kf[i] = *(LAS v4u*)(L + T0 + (64 * w + 16 * (i >> 2) + n16) * 256 + (((4 * (i & 3) + g4) ^ ((n16 & 7) << 1)) << 4));
        PIN_4(kf[0], kf[1], kf[2], kf[3]); PIN_4(kf[4], kf[5], kf[6], kf[7]); PIN_4(kf[8], kf[9], kf[10], kf[11]); PIN_4(kf[12], kf[13], kf[14], kf[15]);
        {
          v16u v0; v8u v1;
          unsigned off = WS_QP + (8 * ga + 2 * w + 1) * 3072 + lane * 16;
          int qw = L_Q + (2 * w + (lane >> 5)) * 288 + (lane & 31) * 8;
          PIN(qw); PIN(off);
          wait_land0(64 - QD);
          issue_q(ws, off, v0, v1);
          finish_q(ws, off, tag, v0, v1);
          const f4 qa_ = (quad<0>(v0) + quad<1>(v0)) + quad<2>(v0), qb_ = (quad<3>(v0) + quad<0>(v1)) + quad<1>(v1);
          float qv[4];
#pragma unroll
          for (int k = 0; k < 4; ++k) {
            auto sw2 = __builtin_amdgcn_permlane32_swap(__float_as_uint(qa_[k]), __float_as_uint(qb_[k]), false, false);
            qv[k] = __uint_as_float(sw2[0]) + __uint_as_float(sw2[1]);
          }
          *(LAS v2u*)(L + qw) = v2u{pk_bf16(qv[0], qv[1]), pk_bf16(qv[2], qv[3])};
          inc_flag(L, F_QC);
        }
        int qa = L_Q + hd * 288 + g4 * 16, pw = pa + w * 1024 + (n16 & 8);
        PIN(qa); PIN(pa); PIN(mla); PIN(pw);
        wait_flag0(F_QC, 4);
        v4u qf[4];
#pragma unroll
        for (int ks = 0; ks < 4; ++ks) qf[ks] = *(LAS v4u*)(L + qa + ks * 64);
        SCHED0();
        f4 s[4];
#pragma unroll
        for (int i = 0; i < 4; ++i) s[i] = f4{0.f, 0.f, 0.f, 0.f};
#pragma unroll
        for (int n = 0; n < 16; ++n) s[n & 3] = mfma32(kf[4 * (n & 3) + (n >> 2)], qf[n >> 2], s[n & 3]);
        SCHED0();
        const f4 sa = hi ? s[1] : s[0], sb = hi ? s[3] : s[2];
        f2 sc[4];
        {
          v2u aa = {pk_bf16(sa[0], sa[1]), pk_bf16(sa[2], sa[3])}, ab = {pk_bf16(sb[0], sb[1]), pk_bf16(sb[2], sb[3])};
          asm("" : "+v"(aa)); asm("" : "+v"(ab));
          const v4u a = {aa.x, aa.y, ab.x, ab.y};
          f2 t[4];
#pragma unroll
          for (int i = 0; i < 4; ++i) t[i] = f2{bf_lo(a[i]), bf_hi(a[i])} * 0.08838834764831845f;
          v2u ba = {pk_bf16(t[0][0], t[0][1]), pk_bf16(t[1][0], t[1][1])}, bb = {pk_bf16(t[2][0], t[2][1]), pk_bf16(t[3][0], t[3][1])};
          asm("" : "+v"(ba)); asm("" : "+v"(bb));
          const v4u a2 = {ba.x, ba.y, bb.x, bb.y};
#pragma unroll
          for (int i = 0; i < 4; ++i) sc[i] = f2{bf_lo(a2[i]), bf_hi(a2[i])};
        }
        float mw = fmx(fmx(fmx(sc[0][0], sc[0][1]), fmx(fmx(sc[1][0], sc[1][1]), sc[2][0])), fmx(fmx(sc[2][1], sc[3][0]), sc[3][1]));
        mw = x32_max(x16_max(fmx(mw, dpp<ROR8>(mw))));
#pragma unroll
        for (int i = 0; i < 4; ++i) sc[i] -= mw;
        SCHED0();
#pragma unroll
        for (int i = 0; i < 4; ++i) sc[i] *= LOG2E;
        SCHED0();
        float p[8];
#pragma unroll
        for (int i = 0; i < 8; ++i) p[i] = __builtin_amdgcn_exp2f(sc[i >> 1][i & 1]);
        *(LAS v2u*)(L + pw) = v2u{pk_bf16(p[0], p[1]), pk_bf16(p[2], p[3])};
        *(LAS v2u*)(L + pw + 512) = v2u{pk_bf16(p[4], p[5]), pk_bf16(p[6], p[7])};
        *(LAS float*)(L + mla + w * 4) = mw;
        inc_flag(L, F_OC);
        float lw = ((p[0] + p[1]) + (p[2] + p[3])) + ((p[4] + p[5]) + (p[6] + p[7]));
        lw = x32_sum(x16_sum(lw + dpp<ROR8>(lw)));
        *(LAS float*)(L + mla + 16 + w * 4) = lw;
        inc_flag(L, F_LC);
        if (w == 0) {
          wait_flag0(F_LC, 4);
          const f4 mu = *(LAS f4*)(L + mla), lu = *(LAS f4*)(L + mla + 16);
          const float M = fmx(fmx(mu[0], mu[1]), fmx(mu[2], mu[3]));
          const float Ls = (__builtin_amdgcn_exp2f((mu[0] - M) * LOG2E) * lu[0] + __builtin_amdgcn_exp2f((mu[1] - M) * LOG2E) * lu[1]) +
                           (__builtin_amdgcn_exp2f((mu[2] - M) * LOG2E) * lu[2] + __builtin_amdgcn_exp2f((mu[3] - M) * LOG2E) * lu[3]);
          wait_flag(F_EP, 8);
          st16m(ws, WS_OP + ((8 * ga + hd) * 16 + sp) * OPS + 512, v4u{__float_as_uint(M), tag, __float_as_uint(Ls), tag}, 0xffull);
        }
      }
    }
    f4 acc[2] = {f4{0.f, 0.f, 0.f, 0.f}, f4{0.f, 0.f, 0.f, 0.f}};
    unsigned po = WS_FP + ((cb * 4 + w) * 32 + h) * 128 + (n16 * 16 + g4 * 4) * 4;
    PIN(po);
    int ha = L_OV + g4 * 16, hb = ha + 256 + (nkd - 2) * 64;
    int da[2] = {ad[0] + wd_reg, ad[1] + wd_reg}, db[2] = {da[0] + (nkd - 2) * 8192, da[1] + (nkd - 2) * 8192};
    unsigned fa = (lane & 2) ? fl1 + ((lane & 1) ? F_HV : F_OV) * 4 : fl2;
    int thr = (lane & 2) ? 1 : n_all;
    PIN(ha); PIN(hb); PIN(da[0]); PIN(da[1]); PIN(db[0]); PIN(db[1]); PIN(fa); PIN(thr);
    v4u wo_f[4][2], wd_f[3][2];
    wait_land(n_wd);
#pragma unroll
    for (int ks = 0; ks < 4; ++ks) { wo_f[ks][0] = trpair(L, ad[0] + 8192 * ks); wo_f[ks][1] = trpair(L, ad[1] + 8192 * ks); }
    wait_land(n_all - 8);
#pragma unroll
    for (int ks = 0; ks < 2; ++ks) { wd_f[1 + ks][0] = trpair(L, da[0] + 8192 * ks); wd_f[1 + ks][1] = trpair(L, da[1] + 8192 * ks); }
    wait_land0(n_all - 4);
    wd_f[0][0] = trpair(L, db[0]); wd_f[0][1] = trpair(L, db[1]);
    wait_vec(fa, thr);
    {
      v4u xf[4];
#pragma unroll
      for (int ks = 0; ks < 4; ++ks) xf[ks] = *(LAS v4u*)(L + ha + ks * 64);
      const v4u x0 = *(LAS v4u*)(L + hb), x2 = *(LAS v4u*)(L + ha + 256), x3 = *(LAS v4u*)(L + ha + 320), x1 = *(LAS v4u*)(L + hb + 64);
      const v4u w0 = trpair(L, db[0] + 8192), w1 = trpair(L, db[1] + 8192);
      SCHED0();
#pragma unroll
      for (int ks = 0; ks < 4; ++ks) { acc[0] = mfma32(wo_f[ks][0], xf[ks], acc[0]); acc[1] = mfma32(wo_f[ks][1], xf[ks], acc[1]); }
      acc[0] = mfma32(wd_f[0][0], x0, acc[0]); acc[1] = mfma32(wd_f[0][1], x0, acc[1]);
      if (!isA) {
        acc[0] = mfma32(wd_f[1][0], x2, acc[0]); acc[1] = mfma32(wd_f[1][1], x2, acc[1]);
        acc[0] = mfma32(wd_f[2][0], x3, acc[0]); acc[1] = mfma32(wd_f[2][1], x3, acc[1]);
      }
      acc[0] = mfma32(w0, x1, acc[0]); acc[1] = mfma32(w1, x1, acc[1]);
    }
    const f4 r = (n16 & 1) ? acc[1] : acc[0];
    st16m(ws, po, enc4(r, tb), 0x0003000300030003ull);
  } else {
    if (isA) {
      const v4u tb = tagbytes(tag);
      const int hd = n16 & 7;
      const bool hi = n16 >= 8;
      int pa = L_P + (g4 * 8 + hd) * 16, mla = L_ML + hd * 32;
      unsigned po = WS_OP + ((8 * ga + hd) * 16 + sp) * OPS + (2 * (w - 4) + (n16 >> 3)) * 64 + g4 * 16;
      int va[2] = {ad[0] + T2, ad[1] + T2};
      PIN(po); PIN(va[0]); PIN(va[1]); PIN(pa); PIN(mla);
      v4u vf[16];
      wait_lds<2, 0>(fl2, 64);
#pragma unroll
      for (int i = 0; i < 16; ++i) vf[i] = trpair(L, va[i & 1] + 8192 * (i >> 1));
      PIN_4(vf[0], vf[1], vf[2], vf[3]); PIN_4(vf[4], vf[5], vf[6], vf[7]); PIN_4(vf[8], vf[9], vf[10], vf[11]); PIN_4(vf[12], vf[13], vf[14], vf[15]);
      wait_flag(F_EP, 8);
      wait_flag(F_QC, 4);
      wait_flag0(F_OC, 4);
      v4u pf[8];
      pf[0] = *(LAS v4u*)(L + pa); pf[1] = *(LAS v4u*)(L + pa + 512);
      const f4 mu = *(LAS f4*)(L + mla);
#pragma unroll
      for (int u = 2; u < 8; ++u) pf[u] = *(LAS v4u*)(L + pa + u * 512);
      SCHED0();
      f4 o[8];
#pragma unroll
      for (int i = 0; i < 8; ++i) o[i] = mfma32(vf[4 * (i >> 1) + 2 + (i & 1)], pf[2 * (i >> 1) + 1], mfma32(vf[4 * (i >> 1) + (i & 1)], pf[2 * (i >> 1)], f4{0.f, 0.f, 0.f, 0.f}));
      const float M = fmx(fmx(mu[0], mu[1]), fmx(mu[2], mu[3]));
      const float e0 = __builtin_amdgcn_exp2f((mu[0] - M) * LOG2E), e1 = __builtin_amdgcn_exp2f((mu[1] - M) * LOG2E),
                  e2 = __builtin_amdgcn_exp2f((mu[2] - M) * LOG2E), e3 = __builtin_amdgcn_exp2f((mu[3] - M) * LOG2E);
      const f4 of = (((hi ? o[1] : o[0]) * e0 + (hi ? o[3] : o[2]) * e1) + (hi ? o[5] : o[4]) * e2) + (hi ? o[7] : o[6]) * e3;
      st16m(ws, po, enc4(of, tb), ~0ull);
    }
    if (w == 4) {
      const v2u tp = tagpair(tag, lane);
      unsigned off = WS_OP + (h * 16 + 4) * OPS + lane * 8, off2 = off + 8 * OPS, loff = WS_OP + (h * 16 + n16) * OPS + 512;
      int ow = L_OV + fragpos(2 * lane);
      PIN_4(off, off2, loff, ow);
      wait_until(t0, T_O);
      v16u v0, v1; v4u ls;
      issue_o(ws, off, off2, loff, v0, v1, ls);
      finish_o(ws, off, off2, loff, tp, tag, v0, v1, ls);
      const float lf = __uint_as_float(ls.x);
      const float e = __builtin_amdgcn_exp2f((lf - row_max(lf)) * LOG2E);
      float ei[16];
#pragma unroll
      for (int i = 0; i < 16; ++i) { int t = __builtin_amdgcn_readlane(__builtin_bit_cast(int, e), i); asm volatile("" : "+s"(t)); ei[i] = __builtin_bit_cast(float, t); }
      const float inv = __builtin_amdgcn_rcpf(row_sum(e * __uint_as_float(ls.z)));
      f2 of = {0.f, 0.f}, og = of;
#pragma unroll
      for (int i = 0; i < 8; ++i) {
        of += f2{__uint_as_float(v0[2 * i]), __uint_as_float(v0[2 * i + 1])} * ei[i];
        og += f2{__uint_as_float(v1[2 * i]), __uint_as_float(v1[2 * i + 1])} * ei[8 + i];
      }
      of = (of + og) * inv;
      *(LAS unsigned*)(L + ow) = pk_bf16(of[0], of[1]);
      set_flag(L, F_OV, 1);
      if (c == 231) {
        GAS u64* p = (GAS u64*)(ws + WS_OP + (8 * g4 * 16 + n16) * OPS + 512);
#pragma nounroll
        for (int n = 0; n < (1 << 20); ++n) {
          if (__all((unsigned)(__hip_atomic_load(p, RLX_AGENT) >> 32) == tag)) break;
          __builtin_amdgcn_s_sleep(8);
        }
        if (lane == 0) __hip_atomic_store((GAS unsigned*)(ws + WS_EPOCH), tag, RLX_AGENT);
      }
    } else if (w == 5) {
      const int e = 32 * kd0 + 4 * ((lane & 31) < 8 * nkd ? (lane & 31) : 0);
      unsigned off = WS_GU + (g * 42 + (lane >> 5) * 21 + 3 * (e >> 7)) * 512 + (e & 127) * 4;
      int hw = L_HV + fragpos((lane & 31) * 4) + (lane >> 5) * 4;
      PIN(hw); PIN(off);
      wait_until(t0, T_H);
      v8u v0; v4u v1;
      issue_h(ws, off, v0, v1);
      finish_h(ws, off, tag, v0, v1);
      const f4 s = (quad<0>(v0) + quad<1>(v0)) + quad<0>(v1);
      float hv[2];
#pragma unroll
      for (int k = 0; k < 2; ++k) {
        auto sw2 = __builtin_amdgcn_permlane32_swap(__float_as_uint(s[k]), __float_as_uint(s[k + 2]), false, false);
        const unsigned gu = pko(__uint_as_float(sw2[0]), __uint_as_float(sw2[1]));
        const float gt = bf_lo(gu);
        hv[k] = rbf(gt * __builtin_amdgcn_rcpf(1.f + __builtin_amdgcn_exp2f(-LOG2E * gt))) * bf_hi(gu);
      }
      *(LAS unsigned*)(L + hw) = pk_bf16(hv[0], hv[1]);
      set_flag(L, F_HV, 1);
    }
  }
  if (isA && w == 7) {
    const int rid = (c & 7) * 8 + (c >> 3);
    const int col = rid * 16 + n16;
    float xres = bf_lo(*(LAS u16*)(L + L_XR + col * 2));
    unsigned tb1 = (tag >> (8 * (lane & 3))) & 255u, oo = col * 2;
    PIN(xres); PIN(tb1); PIN(oo);
    unsigned off = WS_FP + (rid >> 1) * 4096 + g4 * 128 + (rid & 1) * 64 + n16 * 4;
    PIN(off);
    wait_until(t0, T_F);
    v8u v;
    issue_f(ws, off, v);
    finish_f(ws, off, tb1, v);
    const f4 s4v = quad<0>(v) + quad<1>(v);
    float t = (s4v[0] + s4v[1]) + (s4v[2] + s4v[3]);
    t = x32_sum(x16_sum(t)) + xres;
    asm volatile("s_mov_b64 exec, 0xffff\n\tglobal_store_short %0, %1, %2\n\ts_mov_b64 exec, -1" ::"v"(oo), "v"(pk_bf16(t, t)), "s"(out) : "memory");
  }
}

extern "C" void kernel_launch(void* const* d_in, const int* in_sizes, int n_in, void* d_out, int out_size, void* d_ws,
                              size_t ws_size, hipStream_t stream) {
  (void)in_sizes; (void)n_in; (void)out_size; (void)ws_size;
  (void)hipFuncSetAttribute((const void*)fused_decode, hipFuncAttributeMaxDynamicSharedMemorySize, LDS_BYTES);
  hipLaunchKernelGGL(fused_decode, dim3(256), dim3(640), LDS_BYTES, stream, (const u16*)d_in[0], (const u16*)d_in[3],
                     (const u16*)d_in[1], (unsigned char*)d_ws, (const u16*)d_in[2], (const u16*)d_in[6], (const u16*)d_in[7],
                     (const u16*)d_in[4], (const u16*)d_in[8], (u16*)d_out);
}
```
